```python
import torch

def _softmax(x, axis=-1):
    return torch.softmax(x, dim=axis)
def _gelu(x):
    return torch.nn.functional.gelu(x, approximate="tanh")
def _silu(x):
    return torch.nn.functional.silu(x)
def _sigmoid(x):
    return torch.sigmoid(x)
def _layernorm(x, axis=-1, eps=1e-5):
    m = x.mean(dim=axis, keepdim=True)
    v = x.var(dim=axis, keepdim=True, unbiased=False)
    return (x - m) / torch.sqrt(v + eps)
def _scatter_add(tgt, idx, src, axis=0):
    return tgt.clone().index_add_(axis, idx, src)
def _segment_sum(src, seg, n):
    out = torch.zeros((n,) + src.shape[1:], dtype=src.dtype)
    return out.index_add_(0, seg, src)
def _pad(x, pw):
    flat = tuple(v for pair in reversed(pw) for v in pair)
    return torch.nn.functional.pad(x, flat)

kernel_name = 'standalone__gqa_block_cached__near_ridge'

def setup_inputs():

    _w = (lambda a: (lambda t: t.to(torch.bfloat16) if t.is_floating_point() else t)(torch.as_tensor(a)))
    B, Sq, D = 8, 112, 512
    Hq, Hkv, Sk, Dh = 32, 32, 1024, 128
    assert Hq % Hkv == 0, (Hq, Hkv)
    return {
        "x": _w((lambda *s: torch.randn(*s, dtype=torch.bfloat16) * 0.8)(B, Sq, D)),
        "k_cache": _w((lambda *s: torch.randn(*s, dtype=torch.bfloat16) * 0.8)(B, Hkv, Sk, Dh)),
        "v_cache": _w((lambda *s: torch.randn(*s, dtype=torch.bfloat16) * 0.05)(B, Hkv, Sk, Dh)),
        "wq": _w((lambda *s: torch.randn(*s, dtype=torch.bfloat16) * 0.8)(D, Hq * Dh) * (D ** -0.5)),
        "wo": _w((lambda *s: torch.randn(*s, dtype=torch.bfloat16) * 0.05)(Hq * Dh, D) * ((Hq * Dh) ** -0.5)),
    }

def reference(x, k_cache, v_cache, wq, wo):
    B, Sq, D = x.shape
    Hq, Dh = 32, 128
    Hkv = k_cache.shape[1]
    rep = Hq // Hkv
    q = (lambda x, *d: x.permute(*d))((x @ wq).reshape(B, Sq, Hq, Dh), 0, 2, 1, 3)
    k = (lambda x, n, axis: torch.repeat_interleave(x, n, dim=axis))(k_cache, rep, axis=1)
    v = (lambda x, n, axis: torch.repeat_interleave(x, n, dim=axis))(v_cache, rep, axis=1)
    scores = torch.einsum("bhqd,bhkd->bhqk", q, k) * (Dh ** -0.5)
    p = _softmax(scores, axis=-1)
    o = torch.einsum("bhqk,bhkd->bhqd", p, v)
    return (lambda x, *d: x.permute(*d))(o, 0, 2, 1, 3).reshape(B, Sq, Hq * Dh) @ wo
```

```cpp
#include <hip/hip_runtime.h>
#include <hip/hip_bf16.h>
#include <stdint.h>

constexpr int NB = 8, SQ = 112, DM = 512, NH = 32, SK = 1024, DH = 128;
constexpr int NTILE = SK / 64;
constexpr float SL2E = 0.088388347648318447f * 1.4426950408889634f;

using bf16x8 = __attribute__((ext_vector_type(8))) __bf16;
using bf16x4 = __attribute__((ext_vector_type(4))) __bf16;
using s16x4 = __attribute__((ext_vector_type(4))) short;
using s16x8 = __attribute__((ext_vector_type(8))) short;
using f32x16 = __attribute__((ext_vector_type(16))) float;
using f32x4 = __attribute__((ext_vector_type(4))) float;
using u32x2 = __attribute__((ext_vector_type(2))) unsigned;
typedef __attribute__((address_space(3))) s16x4 lds_s16x4;

#define DI __device__ __forceinline__
#define MFMA32(a, b, c) __builtin_amdgcn_mfma_f32_32x32x16_bf16((a), (b), (c), 0, 0, 0)
#define CFENCE() asm volatile("" ::: "memory")
#define BARRIER() do { asm volatile("s_waitcnt lgkmcnt(0)" ::: "memory"); __builtin_amdgcn_s_barrier(); CFENCE(); } while (0)
#define VMCNT(n) asm volatile("s_waitcnt vmcnt(" #n ")" ::: "memory")

DI void glds16(const void* gsrc, unsigned lds_dst) {
  unsigned keep;
  asm volatile("s_mov_b32 %0, m0\n\ts_mov_b32 m0, %2\n\ts_nop 0\n\tglobal_load_lds_dwordx4 %1, off\n\ts_mov_b32 m0, %0"
               : "=&s"(keep) : "v"(gsrc), "s"(lds_dst) : "memory");
}
DI s16x4 tr_read(unsigned addr) {
  return __builtin_amdgcn_ds_read_tr16_b64_v4i16((lds_s16x4*)(uintptr_t)addr);
}
DI bf16x8 cat8(s16x4 lo, s16x4 hi) {
  s16x8 v = __builtin_shufflevector(lo, hi, 0, 1, 2, 3, 4, 5, 6, 7);
  return __builtin_bit_cast(bf16x8, v);
}
DI bf16x8 pack8(const f32x16& x, int s) {
  bf16x8 r;
#pragma unroll
  for (int j = 0; j < 8; ++j) r[j] = (__bf16)x[8 * s + j];
  return r;
}
DI float swap_max(float v) {
  auto rr = __builtin_amdgcn_permlane32_swap(__float_as_uint(v), __float_as_uint(v), false, false);
  return fmaxf(__uint_as_float(rr[0]), __uint_as_float(rr[1]));
}
DI float swap_sum(float v) {
  auto rr = __builtin_amdgcn_permlane32_swap(__float_as_uint(v), __float_as_uint(v), false, false);
  return __uint_as_float(rr[0]) + __uint_as_float(rr[1]);
}

constexpr int LDS_BYTES = 160 * 1024;

__global__ __launch_bounds__(512, 2) void attn_kernel(const __bf16* __restrict__ x, const __bf16* __restrict__ kc,
                                                      const __bf16* __restrict__ vc, const __bf16* __restrict__ wq,
                                                      __bf16* __restrict__ obuf) {
  __shared__ __attribute__((aligned(1024))) char smem[LDS_BYTES];
  const int tid = threadIdx.x, lane = tid & 63;
  const int wid = __builtin_amdgcn_readfirstlane(tid >> 6);
  const int rg = wid & 3, kh = wid >> 2;
  const int r = lane & 31, h = lane >> 5;
  const int bh = blockIdx.x, b = bh >> 5, hd = bh & 31;
  const unsigned lds0 = (unsigned)(uintptr_t)smem;

  const char* Kg = (const char*)(kc + (size_t)bh * SK * DH);
  const char* Vg = (const char*)(vc + (size_t)bh * SK * DH);

  int qrow = rg * 32 + r; if (qrow > SQ - 1) qrow = SQ - 1;
  const __bf16* xrow = x + ((size_t)b * SQ + qrow) * DM + 8 * h;
  bf16x8 xf[32];
#pragma unroll
  for (int s = 0; s < 32; ++s) xf[s] = *reinterpret_cast<const bf16x8*>(xrow + 16 * s);

  const int drow0 = 8 * wid + (lane >> 4), drow1 = drow0 + 4, dc = lane & 15;
  const int koff0 = drow0 * 256 + ((dc ^ (drow0 & 15)) << 4), koff1 = drow1 * 256 + ((dc ^ (drow1 & 15)) << 4);
  const int voff0 = drow0 * 256 + ((dc ^ ((drow0 & 3) << 2)) << 4), voff1 = drow1 * 256 + ((dc ^ ((drow1 & 3) << 2)) << 4);
  const unsigned dpiece = (unsigned)wid * 2048u;
  auto dma_tile = [&](int t, int slot) {
    const unsigned base = lds0 + (unsigned)slot * 32768u + dpiece;
    const char* ks = Kg + (size_t)t * 16384; const char* vs = Vg + (size_t)t * 16384;
    glds16(ks + koff0, base); glds16(ks + koff1, base + 1024);
    glds16(vs + voff0, base + 16384); glds16(vs + voff1, base + 16384 + 1024);
  };
  const char* Wg = (const char*)(wq + hd * DH);
  const int woff0 = drow0 * 8192 + ((dc ^ ((drow0 & 3) << 2)) << 4), woff1 = drow1 * 8192 + ((dc ^ ((drow1 & 3) << 2)) << 4);
  auto dma_wq = [&](int c, int buf) {
    const unsigned base = lds0 + 65536u + (unsigned)buf * 16384u + dpiece;
    const char* ws_ = Wg + (size_t)c * 64 * 8192;
    glds16(ws_ + woff0, base); glds16(ws_ + woff1, base + 1024);
  };
#pragma unroll
  for (int c = 0; c < 6; ++c) dma_wq(c, c);
  dma_tile(0, 0); dma_tile(1, 1);

  const int tq_ = (lane & 15) >> 2, tp = lane & 3, tg = (lane >> 4) & 1;
  const int tpp = ((tp & 1) << 1) | (tp >> 1);
  f32x16 qa[2] = {};
  auto qproj_chunk = [&](int c, int buf) {
    const unsigned base = lds0 + 65536u + (unsigned)buf * 16384u;
#pragma unroll
    for (int s = 0; s < 4; ++s) {
#pragma unroll
      for (int dd = 0; dd < 2; ++dd) {
        const int dt = 2 * kh + dd;
        const int ch = 4 * dt + 2 * tg + (tpp >> 1);
        const int row0 = 16 * s + 8 * h + tq_;
        const unsigned a0 = base + row0 * 256 + ((ch ^ (tq_ << 2)) << 4) + (tpp & 1) * 8;
        s16x4 lo = tr_read(a0), hi = tr_read(a0 + 1024);
        qa[dd] = MFMA32(cat8(lo, hi), xf[4 * c + s], qa[dd]);
      }
    }
  };
  VMCNT(18); BARRIER(); qproj_chunk(0, 0);
  VMCNT(16); BARRIER(); dma_wq(6, 0); qproj_chunk(1, 1);
  VMCNT(16); BARRIER(); dma_wq(7, 1); qproj_chunk(2, 2);
  VMCNT(16); BARRIER(); qproj_chunk(3, 3);
  VMCNT(14); BARRIER(); qproj_chunk(4, 4);
  VMCNT(12); BARRIER(); qproj_chunk(5, 5);
  VMCNT(2); BARRIER(); qproj_chunk(6, 0);
  VMCNT(0); BARRIER(); qproj_chunk(7, 1);
  {
    char* qx = smem + 131072 + rg * 8192 + lane * 16;
#pragma unroll
    for (int dd = 0; dd < 2; ++dd)
#pragma unroll
      for (int s = 0; s < 2; ++s) *reinterpret_cast<bf16x8*>(qx + ((2 * kh + dd) * 2 + s) * 1024) = pack8(qa[dd], s);
  }
  BARRIER();
  dma_tile(2, 2);
  bf16x8 qf[8];
  {
    const char* qx = smem + 131072 + rg * 8192 + lane * 16;
#pragma unroll
    for (int f = 0; f < 8; ++f) qf[f] = *reinterpret_cast<const bf16x8*>(qx + f * 1024);
  }

  float m_run = -__builtin_inff(), l_run = 0.f;
  f32x16 o[4] = {};
  const int krow = 32 * kh + r;
  const unsigned kaddr = krow * 256;
  const int ksw = krow & 15;
  const unsigned vaddr = 16384 + (32 * kh + 4 * h + tq_) * 256 + (tp & 1) * 8;
  for (int t = 0; t < NTILE; ++t) {
    if (t < NTILE - 2) { VMCNT(8); } else if (t == NTILE - 2) { VMCNT(4); } else { VMCNT(0); }
    BARRIER();
    if (t + 3 < NTILE) dma_tile(t + 3, (t + 3) & 3);
    const unsigned slot = lds0 + (unsigned)(t & 3) * 32768u;
    f32x16 sacc = {};
#pragma unroll
    for (int s = 0; s < 8; ++s) {
      const bf16x8 kf = *reinterpret_cast<const bf16x8*>(smem + (slot - lds0) + kaddr + (((2 * s + h) ^ ksw) << 4));
      sacc = MFMA32(kf, qf[s], sacc);
    }
    float pmax = sacc[0];
#pragma unroll
    for (int i = 1; i < 16; ++i) pmax = fmaxf(pmax, sacc[i]);
    pmax = swap_max(pmax) * SL2E;
    if (!__all(pmax <= m_run)) {
      const float mn = fmaxf(m_run, pmax);
      const float alpha = __builtin_amdgcn_exp2f(m_run - mn);
      m_run = mn; l_run *= alpha;
#pragma unroll
      for (int d = 0; d < 4; ++d)
#pragma unroll
        for (int i = 0; i < 16; ++i) o[d][i] *= alpha;
    }
    float ps = 0.f;
#pragma unroll
    for (int i = 0; i < 16; ++i) { sacc[i] = __builtin_amdgcn_exp2f(fmaf(sacc[i], SL2E, -m_run)); ps += sacc[i]; }
    l_run += ps;
    const bf16x8 p0 = pack8(sacc, 0), p1 = pack8(sacc, 1);
#pragma unroll
    for (int dt = 0; dt < 4; ++dt) {
#pragma unroll
      for (int s = 0; s < 2; ++s) {
        const unsigned a0 = slot + vaddr + s * 4096 + (((4 * dt + 2 * tg + (tp >> 1)) ^ (tq_ << 2)) << 4);
        s16x4 lo = tr_read(a0), hi = tr_read(a0 + 2048);
        o[dt] = MFMA32(cat8(lo, hi), s ? p1 : p0, o[dt]);
      }
    }
  }
  l_run = swap_sum(l_run);
  BARRIER();
  float* xo = reinterpret_cast<float*>(smem) + rg * 4096 * 4 / 4;
  float* xml = reinterpret_cast<float*>(smem + 65536) + rg * 128;
  if (kh == 1) {
#pragma unroll
    for (int d = 0; d < 4; ++d)
#pragma unroll
      for (int g = 0; g < 4; ++g) {
        f32x4 v = {o[d][4 * g], o[d][4 * g + 1], o[d][4 * g + 2], o[d][4 * g + 3]};
        *reinterpret_cast<f32x4*>(xo + ((d * 4 + g) * 64 + lane) * 4) = v;
      }
    xml[lane] = m_run; xml[64 + lane] = l_run;
  }
  BARRIER();
  if (kh == 0) {
    const float m2 = xml[lane], l2 = xml[64 + lane];
    const float mn = fmaxf(m_run, m2);
    const float a1 = __builtin_amdgcn_exp2f(m_run - mn), a2 = __builtin_amdgcn_exp2f(m2 - mn);
    const float inv = 1.f / (l_run * a1 + l2 * a2);
    const float s1 = a1 * inv, s2 = a2 * inv;
    const int q = rg * 32 + r;
    if (q < SQ) {
      __bf16* orow = obuf + ((size_t)b * SQ + q) * (NH * DH) + hd * DH + 4 * h;
#pragma unroll
      for (int d = 0; d < 4; ++d)
#pragma unroll
        for (int g = 0; g < 4; ++g) {
          const f32x4 v = *reinterpret_cast<const f32x4*>(xo + ((d * 4 + g) * 64 + lane) * 4);
          bf16x4 w;
#pragma unroll
          for (int j = 0; j < 4; ++j) w[j] = (__bf16)(o[d][4 * g + j] * s1 + v[j] * s2);
          *reinterpret_cast<bf16x4*>(orow + d * 32 + g * 8) = w;
        }
    }
  }
}

__global__ __launch_bounds__(256) void oproj_kernel(const __bf16* __restrict__ obuf, const __bf16* __restrict__ wo,
                                                    __bf16* __restrict__ out) {
  __shared__ float red[4][16][64];
  const int tid = threadIdx.x, lane = tid & 63, wid = tid >> 6, r = lane & 31, h = lane >> 5;
  const int mt = blockIdx.x >> 4, nt = blockIdx.x & 15;
  const __bf16* arow = obuf + (size_t)(mt * 32 + r) * 4096 + wid * 1024 + 8 * h;
  const __bf16* bcol = wo + (size_t)(wid * 1024 + 8 * h) * 512 + nt * 32 + r;
  f32x16 acc = {};
  for (int s = 0; s < 64; ++s) {
    bf16x8 a = *reinterpret_cast<const bf16x8*>(arow + 16 * s);
    bf16x8 bb;
#pragma unroll
    for (int j = 0; j < 8; ++j) bb[j] = bcol[(size_t)(16 * s + j) * 512];
    acc = MFMA32(a, bb, acc);
  }
#pragma unroll
  for (int i = 0; i < 16; ++i) red[wid][i][lane] = acc[i];
  __syncthreads();
  if (wid == 0) {
#pragma unroll
    for (int i = 0; i < 16; ++i) {
      const float v = red[0][i][lane] + red[1][i][lane] + red[2][i][lane] + red[3][i][lane];
      const int row = (i & 3) + 8 * (i >> 2) + 4 * h;
      out[(size_t)(mt * 32 + row) * 512 + nt * 32 + r] = (__bf16)v;
    }
  }
}

extern "C" void kernel_launch(void* const* d_in, const int* in_sizes, int n_in, void* d_out, int out_size, void* d_ws,
                              size_t ws_size, hipStream_t stream) {
  const __bf16* x = (const __bf16*)d_in[0];
  const __bf16* kc = (const __bf16*)d_in[1];
  const __bf16* vc = (const __bf16*)d_in[2];
  const __bf16* wq = (const __bf16*)d_in[3];
  const __bf16* wo = (const __bf16*)d_in[4];
  __bf16* obuf = (__bf16*)d_ws;
  attn_kernel<<<NB * NH, 512, 0, stream>>>(x, kc, vc, wq, obuf);
  oproj_kernel<<<28 * 16, 256, 0, stream>>>(obuf, wo, (__bf16*)d_out);
}
```

```cpp
#include <hip/hip_runtime.h>
#include <hip/hip_bf16.h>
#include <stdint.h>

constexpr int NB = 8, SQ = 112, DM = 512, NH = 32, SK = 1024, DH = 128;
constexpr int NTILE = SK / 64;
constexpr float SL2E = 0.088388347648318447f * 1.4426950408889634f;

using bf16x8 = __attribute__((ext_vector_type(8))) __bf16;
using bf16x4 = __attribute__((ext_vector_type(4))) __bf16;
using s16x4 = __attribute__((ext_vector_type(4))) short;
using s16x8 = __attribute__((ext_vector_type(8))) short;
using f32x16 = __attribute__((ext_vector_type(16))) float;
using f32x4 = __attribute__((ext_vector_type(4))) float;
using u32x2 = __attribute__((ext_vector_type(2))) unsigned;
using u32x4 = __attribute__((ext_vector_type(4))) unsigned;
typedef __attribute__((address_space(3))) s16x4 lds_s16x4;

#define DI __device__ __forceinline__
#define MFMA32(a, b, c) __builtin_amdgcn_mfma_f32_32x32x16_bf16((a), (b), (c), 0, 0, 0)
#define CFENCE() asm volatile("" ::: "memory")
#define BARRIER() do { asm volatile("s_waitcnt lgkmcnt(0)" ::: "memory"); __builtin_amdgcn_s_barrier(); CFENCE(); } while (0)
#define VMCNT(n) asm volatile("s_waitcnt vmcnt(" #n ")" ::: "memory")

DI void glds16(const void* gsrc, unsigned lds_dst) {
  unsigned keep;
  asm volatile("s_mov_b32 %0, m0\n\ts_mov_b32 m0, %2\n\ts_nop 0\n\tglobal_load_lds_dwordx4 %1, off\n\ts_mov_b32 m0, %0"
               : "=&s"(keep) : "v"(gsrc), "s"(lds_dst) : "memory");
}
DI void glds16nt(const void* gsrc, unsigned lds_dst) {
  unsigned keep;
  asm volatile("s_mov_b32 %0, m0\n\ts_mov_b32 m0, %2\n\ts_nop 0\n\tglobal_load_lds_dwordx4 %1, off nt\n\ts_mov_b32 m0, %0"
               : "=&s"(keep) : "v"(gsrc), "s"(lds_dst) : "memory");
}
DI s16x4 tr_read(unsigned addr) {
  return __builtin_amdgcn_ds_read_tr16_b64_v4i16((lds_s16x4*)(uintptr_t)addr);
}
DI bf16x8 cat8(s16x4 lo, s16x4 hi) {
  s16x8 v = __builtin_shufflevector(lo, hi, 0, 1, 2, 3, 4, 5, 6, 7);
  return __builtin_bit_cast(bf16x8, v);
}
DI bf16x8 pack8(const f32x16& x, int s) {
  bf16x8 r;
#pragma unroll
  for (int j = 0; j < 8; ++j) r[j] = (__bf16)x[8 * s + j];
  return r;
}
DI float swap_max(float v) {
  auto rr = __builtin_amdgcn_permlane32_swap(__float_as_uint(v), __float_as_uint(v), false, false);
  return fmaxf(__uint_as_float(rr[0]), __uint_as_float(rr[1]));
}
DI float swap_sum(float v) {
  auto rr = __builtin_amdgcn_permlane32_swap(__float_as_uint(v), __float_as_uint(v), false, false);
  return __uint_as_float(rr[0]) + __uint_as_float(rr[1]);
}

constexpr int LDS_BYTES = 160 * 1024;
#define STAMP(i)

__global__ __launch_bounds__(512, 2) void attn_kernel(const __bf16* __restrict__ x, const __bf16* __restrict__ kc,
                                                      const __bf16* __restrict__ vc, const __bf16* __restrict__ wq,
                                                      __bf16* __restrict__ obuf, unsigned* __restrict__ cnt) {
  __shared__ __attribute__((aligned(1024))) char smem[LDS_BYTES];
  const int tid = threadIdx.x, lane = tid & 63;
  if (blockIdx.x == 0 && tid < 64) cnt[tid] = 0u;
  const int wid = __builtin_amdgcn_readfirstlane(tid >> 6);
  const int rg = wid & 3, kh = wid >> 2;
  const int r = lane & 31, h = lane >> 5;
  const int bh = blockIdx.x, b = bh >> 5, hd = bh & 31;
  const unsigned lds0 = (unsigned)(uintptr_t)smem;

  const char* Kg = (const char*)(kc + (size_t)bh * SK * DH);
  const char* Vg = (const char*)(vc + (size_t)bh * SK * DH);
  STAMP(0);
  const int dc = lane & 15;
  const char* Wg = (const char*)(wq + hd * DH);
  const char* Xg = (const char*)(x + (size_t)b * SQ * DM);
  auto dma_stage = [&](int c, int reg) {
    const unsigned base = lds0 + (unsigned)reg * 32768u + (unsigned)rg * 4096u;
#pragma unroll
    for (int p = 0; p < 4; ++p) {
      const int xr = 32 * rg + 8 * p + (lane >> 3), xc = lane & 7;
      glds16(Xg + c * 128 + min(xr, SQ - 1) * 1024 + ((xc ^ ((xr >> 1) & 7)) << 4), base + p * 1024);
    }
#pragma unroll
    for (int p = 0; p < 4; ++p) {
      const int wr = 16 * rg + 4 * p + (lane >> 4);
      glds16(Wg + (size_t)(c * 64 + wr) * 8192 + ((dc ^ ((wr & 3) << 2)) << 4), base + 16384 + p * 1024);
    }
  };
  u32x4 pre[3][8];
  auto kv_rows = [&](int t, int p, const char*& ks, const char*& vs) {
    const int row = 16 * rg + 4 * p + (lane >> 4);
    ks = Kg + (size_t)t * 16384 + row * 256 + ((dc ^ (row & 15)) << 4);
    vs = Vg + (size_t)t * 16384 + row * 256 + ((dc ^ ((row & 3) << 2)) << 4);
  };
  auto kv_dma4 = [&](int t) {
#pragma unroll
    for (int p = 0; p < 4; ++p) {
      const char *ks, *vs; kv_rows(t, p, ks, vs);
      const unsigned base = lds0 + (unsigned)t * 32768u + (unsigned)rg * 4096u + p * 1024;
      glds16nt(ks, base); glds16nt(vs, base + 16384);
    }
  };
  auto kv_reg4 = [&](int i) {
#pragma unroll
    for (int p = 0; p < 4; ++p) {
      const char *ks, *vs; kv_rows(2 + i, p, ks, vs);
      pre[i][p] = __builtin_nontemporal_load(reinterpret_cast<const u32x4*>(ks));
      pre[i][4 + p] = __builtin_nontemporal_load(reinterpret_cast<const u32x4*>(vs));
    }
  };
  unsigned tch[8] = {0, 0, 0, 0, 0, 0, 0, 0};
  if (kh == 0) {
    dma_stage(0, 2); dma_stage(1, 3); dma_stage(2, 4);
  } else {
    kv_dma4(0); kv_dma4(1);
  }
  auto put_pre = [&](int i, int reg) {
    char* dst = smem + reg * 32768 + rg * 4096 + lane * 16;
#pragma unroll
    for (int p = 0; p < 4; ++p) {
      *reinterpret_cast<u32x4*>(dst + p * 1024) = pre[i][p];
      *reinterpret_cast<u32x4*>(dst + 16384 + p * 1024) = pre[i][4 + p];
    }
  };

  const int tq_ = (lane & 15) >> 2, tp = lane & 3, tg = (lane >> 4) & 1;
  const int tpp = ((tp & 1) << 1) | (tp >> 1);
  f32x16 qa[2] = {};
  const int xrow = rg * 32 + r;
  auto qproj_chunk = [&](int reg) {
    const unsigned base = lds0 + (unsigned)reg * 32768u;
#pragma unroll
    for (int s = 0; s < 4; ++s) {
      const bf16x8 xf = *reinterpret_cast<const bf16x8*>(smem + reg * 32768 + xrow * 128 + (((2 * s + h) ^ ((xrow >> 1) & 7)) << 4));
#pragma unroll
      for (int dd = 0; dd < 2; ++dd) {
        const int dt = 2 * kh + dd;
        const int ch = 4 * dt + 2 * tg + (tpp >> 1);
        const int row0 = 16 * s + 8 * h + tq_;
        const unsigned a0 = base + 16384 + row0 * 256 + ((ch ^ (tq_ << 2)) << 4) + (tpp & 1) * 8;
        s16x4 lo = tr_read(a0), hi = tr_read(a0 + 1024);
        qa[dd] = MFMA32(cat8(lo, hi), xf, qa[dd]);
      }
    }
  };
  if (kh == 0) VMCNT(16);
  asm volatile("" :: "v"(tch[0]), "v"(tch[1]), "v"(tch[2]), "v"(tch[3]), "v"(tch[4]), "v"(tch[5]), "v"(tch[6]), "v"(tch[7]));
  BARRIER(); STAMP(1); qproj_chunk(2);
  if (kh == 0) VMCNT(8);
  BARRIER(); if (kh == 0) dma_stage(3, 2); else kv_reg4(0); qproj_chunk(3);
  if (kh == 0) VMCNT(8);
  BARRIER(); if (kh == 0) dma_stage(4, 3); qproj_chunk(4);
  if (kh == 0) VMCNT(8);
  BARRIER(); if (kh == 0) dma_stage(5, 4); else kv_reg4(1); qproj_chunk(2);
  if (kh == 0) VMCNT(8);
  BARRIER(); if (kh == 0) dma_stage(6, 2); qproj_chunk(3);
  if (kh == 0) VMCNT(8);
  BARRIER(); if (kh == 0) dma_stage(7, 3); else kv_reg4(2); qproj_chunk(4);
  if (kh == 0) VMCNT(8);
  BARRIER(); qproj_chunk(2);
  if (kh == 0) VMCNT(0);
  BARRIER(); qproj_chunk(3);
  {
    char* qx = smem + 131072 + rg * 8192 + lane * 16;
#pragma unroll
    for (int dd = 0; dd < 2; ++dd)
#pragma unroll
      for (int s = 0; s < 2; ++s) *reinterpret_cast<bf16x8*>(qx + ((2 * kh + dd) * 2 + s) * 1024) = pack8(qa[dd], s);
  }
  BARRIER();
  STAMP(2);
  bf16x8 qf[8];
  {
    const char* qx = smem + 131072 + rg * 8192 + lane * 16;
#pragma unroll
    for (int f = 0; f < 8; ++f) qf[f] = *reinterpret_cast<const bf16x8*>(qx + f * 1024);
  }
  if (kh == 1) { put_pre(0, 2); put_pre(1, 3); VMCNT(0); }
  BARRIER();
  if (kh == 1) put_pre(2, 4);

  const int drow0 = 8 * wid + (lane >> 4), drow1 = drow0 + 4;
  const int koff0 = drow0 * 256 + ((dc ^ (drow0 & 15)) << 4), koff1 = drow1 * 256 + ((dc ^ (drow1 & 15)) << 4);
  const int voff0 = drow0 * 256 + ((dc ^ ((drow0 & 3) << 2)) << 4), voff1 = drow1 * 256 + ((dc ^ ((drow1 & 3) << 2)) << 4);
  auto dma_tile = [&](int t, int slot) {
    const unsigned base = lds0 + (unsigned)slot * 32768u + (unsigned)wid * 2048u;
    const char* ks = Kg + (size_t)t * 16384; const char* vs = Vg + (size_t)t * 16384;
    glds16nt(ks + koff0, base); glds16nt(ks + koff1, base + 1024);
    glds16nt(vs + voff0, base + 16384); glds16nt(vs + voff1, base + 16384 + 1024);
  };
  f32x16 o[4] = {};
  const int krow = 32 * kh + r;
  const unsigned kaddr = krow * 256;
  const int ksw = krow & 15;
  const unsigned vaddr = 16384 + (32 * kh + 4 * h + tq_) * 256 + (tp & 1) * 8;
  auto rowmax = [&](const f32x16& sv) {
    float pm = sv[0];
#pragma unroll
    for (int i = 1; i < 16; ++i) pm = fmaxf(pm, sv[i]);
    return swap_max(pm) * SL2E;
  };
  f32x16 scur = {};
#pragma unroll
  for (int s = 0; s < 8; ++s)
    scur = MFMA32(*reinterpret_cast<const bf16x8*>(smem + kaddr + (((2 * s + h) ^ ksw) << 4)), qf[s], scur);
  float m_run = rowmax(scur), l_run = 0.f, alpha = 1.f;
  int cur = 0;
  STAMP(3);
  for (int t = 0; t < NTILE; ++t) {
    if (t < NTILE - 3) { VMCNT(8); } else if (t == NTILE - 3) { VMCNT(4); } else { VMCNT(0); }
    BARRIER();
    const int nxt = cur == 4 ? 0 : cur + 1;
    if (t >= 1 && t + 4 < NTILE) dma_tile(t + 4, cur == 0 ? 4 : cur - 1);
    const unsigned slot = lds0 + (unsigned)cur * 32768u;
    bf16x8 kf[8];
    s16x4 vlo[8], vhi[8];
#pragma unroll
    for (int s = 0; s < 8; ++s) kf[s] = *reinterpret_cast<const bf16x8*>(smem + nxt * 32768 + kaddr + (((2 * s + h) ^ ksw) << 4));
#pragma unroll
    for (int dt = 0; dt < 4; ++dt)
#pragma unroll
      for (int s = 0; s < 2; ++s) {
        const unsigned a0 = slot + vaddr + s * 4096 + (((4 * dt + 2 * tg + (tp >> 1)) ^ (tq_ << 2)) << 4);
        vlo[2 * dt + s] = tr_read(a0); vhi[2 * dt + s] = tr_read(a0 + 2048);
      }
    __builtin_amdgcn_sched_barrier(0);
    f32x16 snext = {};
#pragma unroll
    for (int s = 0; s < 8; ++s) snext = MFMA32(kf[s], qf[s], snext);
#pragma unroll
    for (int d = 0; d < 4; ++d)
#pragma unroll
      for (int i = 0; i < 16; ++i) o[d][i] *= alpha;
    float ps = 0.f;
#pragma unroll
    for (int i = 0; i < 16; ++i) { scur[i] = __builtin_amdgcn_exp2f(fmaf(scur[i], SL2E, -m_run)); ps += scur[i]; }
    l_run = fmaf(l_run, alpha, ps);
    const bf16x8 p0 = pack8(scur, 0), p1 = pack8(scur, 1);
#pragma unroll
    for (int dt = 0; dt < 4; ++dt) {
      o[dt] = MFMA32(cat8(vlo[2 * dt], vhi[2 * dt]), p0, o[dt]);
      o[dt] = MFMA32(cat8(vlo[2 * dt + 1], vhi[2 * dt + 1]), p1, o[dt]);
    }
    const float pmax = rowmax(snext);
    {
      const float mn = (t < NTILE - 1) ? fmaxf(m_run, pmax) : m_run;
      alpha = __builtin_amdgcn_exp2f(m_run - mn);
      m_run = mn;
    }
    scur = snext;
    cur = nxt;
  }
  l_run = swap_sum(l_run);
  BARRIER();
  STAMP(19);
  {
    float* xo = reinterpret_cast<float*>(smem) + wid * 2048;
    float* xml = reinterpret_cast<float*>(smem + 65536) + wid * 128;
#pragma unroll
    for (int dd = 0; dd < 2; ++dd)
#pragma unroll
      for (int g = 0; g < 4; ++g) {
        f32x4 v;
#pragma unroll
        for (int j = 0; j < 4; ++j) v[j] = kh ? o[dd][4 * g + j] : o[2 + dd][4 * g + j];
        *reinterpret_cast<f32x4*>(xo + ((dd * 4 + g) * 64 + lane) * 4) = v;
      }
    xml[lane] = m_run; xml[64 + lane] = l_run;
  }
  BARRIER();
  {
    const float* xo = reinterpret_cast<const float*>(smem) + (wid ^ 4) * 2048;
    const float* xml = reinterpret_cast<const float*>(smem + 65536) + (wid ^ 4) * 128;
    const float m2 = xml[lane], l2 = xml[64 + lane];
    const float mn = fmaxf(m_run, m2);
    const float a1 = __builtin_amdgcn_exp2f(m_run - mn), a2 = __builtin_amdgcn_exp2f(m2 - mn);
    const float inv = 1.f / (l_run * a1 + l2 * a2);
    const float s1 = a1 * inv, s2 = a2 * inv;
    char* ot = smem + 98304 + xrow * 256;
#pragma unroll
    for (int dd = 0; dd < 2; ++dd)
#pragma unroll
      for (int g = 0; g < 4; ++g) {
        const int dt = 2 * kh + dd;
        const f32x4 v = *reinterpret_cast<const f32x4*>(xo + ((dd * 4 + g) * 64 + lane) * 4);
        bf16x4 w;
#pragma unroll
        for (int j = 0; j < 4; ++j) w[j] = (__bf16)((kh ? o[2 + dd][4 * g + j] : o[dd][4 * g + j]) * s1 + v[j] * s2);
        *reinterpret_cast<bf16x4*>(ot + (((4 * dt + g) ^ (xrow & 15)) << 4) + h * 8) = w;
      }
  }
  BARRIER();
  {
    char* og = (char*)(obuf + (size_t)b * SQ * (NH * DH) + hd * DH);
#pragma unroll
    for (int i = 0; i < 4; ++i) {
      const int idx = tid + i * 512;
      if (idx < SQ * 16) {
        const int row = idx >> 4, ch = idx & 15;
        const u32x4 v = *reinterpret_cast<const u32x4*>(smem + 98304 + row * 256 + ((ch ^ (row & 15)) << 4));
        *reinterpret_cast<u32x4*>(og + (size_t)row * 8192 + ch * 16) = v;
      }
    }
  }
  STAMP(20);
}

constexpr int OP_STAGES = 6, OP_STAGE_BYTES = 24576;
__global__ __launch_bounds__(512, 2) void oproj_kernel(const __bf16* __restrict__ obuf, const __bf16* __restrict__ wo,
                                                       __bf16* __restrict__ out, float* __restrict__ slabs,
                                                       unsigned* __restrict__ cnt) {
  __shared__ __attribute__((aligned(1024))) char smem[OP_STAGES * OP_STAGE_BYTES + 64];
  const int tid = threadIdx.x, lane = tid & 63;
  const int wid = __builtin_amdgcn_readfirstlane(tid >> 6);
  const int rg = wid & 3, nh = wid >> 2, r = lane & 31, h = lane >> 5;
  const int id = blockIdx.x, j_ = id >> 3, ks = j_ & 3, tile = (id & 7) + 8 * (j_ >> 2), b = tile >> 3, nt = tile & 7;
  const unsigned lds0 = (unsigned)(uintptr_t)smem;
  const char* Ag = (const char*)(obuf + (size_t)b * SQ * 4096 + ks * 1024);
  const char* Bg = (const char*)(wo + (size_t)ks * 1024 * 512 + nt * 64);
  const int ar0 = 16 * wid + (lane >> 3), ar1 = ar0 + 8, ac = lane & 7, br = 8 * wid + (lane >> 3);
  const int aoff0 = min(ar0, SQ - 1) * 8192 + ((ac ^ ((ar0 >> 1) & 7)) << 4);
  const int aoff1 = min(ar1, SQ - 1) * 8192 + ((ac ^ ((ar1 >> 1) & 7)) << 4);
  const int boff = br * 1024 + ((ac ^ (((br >> 1) & 1) << 2)) << 4);
  auto dma_stage = [&](int c, int st) {
    const unsigned base = lds0 + (unsigned)st * OP_STAGE_BYTES;
    glds16(Ag + c * 128 + aoff0, base + wid * 2048); glds16(Ag + c * 128 + aoff1, base + wid * 2048 + 1024);
    glds16(Bg + (size_t)c * 65536 + boff, base + 16384 + wid * 1024);
  };
#pragma unroll
  for (int c = 0; c < OP_STAGES - 1; ++c) dma_stage(c, c);
  const int tq_ = (lane & 15) >> 2, tp = lane & 3, tg = (lane >> 4) & 1;
  const int arow = rg * 32 + r;
  f32x16 acc = {};
  int st = 0;
  for (int c = 0; c < 16; ++c) {
    if (c < 16 - (OP_STAGES - 2)) { VMCNT(12); } else if (c == 12) { VMCNT(9); } else if (c == 13) { VMCNT(6); } else if (c == 14) { VMCNT(3); } else { VMCNT(0); }
    BARRIER();
    if (c + OP_STAGES - 1 < 16) dma_stage(c + OP_STAGES - 1, st == 0 ? OP_STAGES - 1 : st - 1);
    const char* sb = smem + st * OP_STAGE_BYTES;
    const unsigned bb = lds0 + (unsigned)st * OP_STAGE_BYTES + 16384;
#pragma unroll
    for (int s = 0; s < 4; ++s) {
      const bf16x8 af = *reinterpret_cast<const bf16x8*>(sb + arow * 128 + (((2 * s + h) ^ ((arow >> 1) & 7)) << 4));
      const int row0 = 16 * s + 8 * h + tq_;
      const unsigned a0 = bb + row0 * 128 + (((4 * nh + 2 * tg + (tp >> 1)) ^ (((row0 >> 1) & 1) << 2)) << 4) + (tp & 1) * 8;
      s16x4 lo = tr_read(a0), hi = tr_read(a0 + 512);
      acc = MFMA32(af, cat8(lo, hi), acc);
    }
    st = st == OP_STAGES - 1 ? 0 : st + 1;
  }
  float* slab = slabs + ((size_t)tile * 4) * (128 * 64);
  {
    float* my = slab + (size_t)ks * (128 * 64) + nh * 32 + r;
#pragma unroll
    for (int i = 0; i < 16; ++i) my[(rg * 32 + (i & 3) + 8 * (i >> 2) + 4 * h) * 64] = acc[i];
  }
  VMCNT(0);
  __syncthreads();
  unsigned* flag = reinterpret_cast<unsigned*>(smem + OP_STAGES * OP_STAGE_BYTES);
  if (tid == 0) {
    __builtin_amdgcn_fence(__ATOMIC_RELEASE, "agent");
    VMCNT(0);
    const unsigned tk = __hip_atomic_fetch_add(cnt + tile, 1u, __ATOMIC_RELAXED, __HIP_MEMORY_SCOPE_AGENT);
    if (tk == 3) { __builtin_amdgcn_fence(__ATOMIC_ACQUIRE, "agent"); VMCNT(0); }
    *flag = tk;
  }
  __syncthreads();
  if (*flag != 3) return;
  for (int idx = tid; idx < SQ * 16; idx += 512) {
    const int row = idx >> 4, c4 = (idx & 15) * 4;
    f32x4 v = *reinterpret_cast<const f32x4*>(slab + row * 64 + c4);
#pragma unroll
    for (int k = 1; k < 4; ++k) v += *reinterpret_cast<const f32x4*>(slab + (size_t)k * (128 * 64) + row * 64 + c4);
    bf16x4 w;
#pragma unroll
    for (int j = 0; j < 4; ++j) w[j] = (__bf16)v[j];
    *reinterpret_cast<bf16x4*>(out + ((size_t)b * SQ + row) * 512 + nt * 64 + c4) = w;
  }
}

extern "C" void kernel_launch(void* const* d_in, const int* in_sizes, int n_in, void* d_out, int out_size, void* d_ws,
                              size_t ws_size, hipStream_t stream) {
  const __bf16* x = (const __bf16*)d_in[0];
  const __bf16* kc = (const __bf16*)d_in[1];
  const __bf16* vc = (const __bf16*)d_in[2];
  const __bf16* wq = (const __bf16*)d_in[3];
  const __bf16* wo = (const __bf16*)d_in[4];
  __bf16* obuf = (__bf16*)d_ws;
  float* slabs = (float*)((char*)d_ws + (16u << 20));
  unsigned* cnt = (unsigned*)((char*)d_ws + (32u << 20));
  attn_kernel<<<NB * NH, 512, 0, stream>>>(x, kc, vc, wq, obuf, cnt);
  oproj_kernel<<<256, 512, 0, stream>>>(obuf, wo, (__bf16*)d_out, slabs, cnt);
}
```

```cpp
#include <hip/hip_runtime.h>
#include <hip/hip_bf16.h>
#include <stdint.h>

constexpr int NB = 8, SQ = 112, DM = 512, NH = 32, SK = 1024, DH = 128;
constexpr int NTILE = SK / 64;
constexpr float SL2E = 0.088388347648318447f * 1.4426950408889634f;

using bf16x8 = __attribute__((ext_vector_type(8))) __bf16;
using bf16x4 = __attribute__((ext_vector_type(4))) __bf16;
using s16x4 = __attribute__((ext_vector_type(4))) short;
using s16x8 = __attribute__((ext_vector_type(8))) short;
using f32x16 = __attribute__((ext_vector_type(16))) float;
using f32x4 = __attribute__((ext_vector_type(4))) float;
using u32x2 = __attribute__((ext_vector_type(2))) unsigned;
using u32x4 = __attribute__((ext_vector_type(4))) unsigned;
typedef __attribute__((address_space(3))) s16x4 lds_s16x4;

#define DI __device__ __forceinline__
#define MFMA32(a, b, c) __builtin_amdgcn_mfma_f32_32x32x16_bf16((a), (b), (c), 0, 0, 0)
#define CFENCE() asm volatile("" ::: "memory")
#define BARRIER() do { asm volatile("s_waitcnt lgkmcnt(0)" ::: "memory"); __builtin_amdgcn_s_barrier(); CFENCE(); } while (0)
#define VMCNT(n) asm volatile("s_waitcnt vmcnt(" #n ")" ::: "memory")

DI void glds16(const void* gsrc, unsigned lds_dst) {
  unsigned keep;
  asm volatile("s_mov_b32 %0, m0\n\ts_mov_b32 m0, %2\n\ts_nop 0\n\tglobal_load_lds_dwordx4 %1, off\n\ts_mov_b32 m0, %0"
               : "=&s"(keep) : "v"(gsrc), "s"(lds_dst) : "memory");
}
DI void glds16nt(const void* gsrc, unsigned lds_dst) {
  unsigned keep;
  asm volatile("s_mov_b32 %0, m0\n\ts_mov_b32 m0, %2\n\ts_nop 0\n\tglobal_load_lds_dwordx4 %1, off nt\n\ts_mov_b32 m0, %0"
               : "=&s"(keep) : "v"(gsrc), "s"(lds_dst) : "memory");
}
DI void glds16sc1(const void* gsrc, unsigned lds_dst) {
  unsigned keep;
  asm volatile("s_mov_b32 %0, m0\n\ts_mov_b32 m0, %2\n\ts_nop 0\n\tglobal_load_lds_dwordx4 %1, off sc1\n\ts_mov_b32 m0, %0"
               : "=&s"(keep) : "v"(gsrc), "s"(lds_dst) : "memory");
}
DI s16x4 tr_read(unsigned addr) {
  return __builtin_amdgcn_ds_read_tr16_b64_v4i16((lds_s16x4*)(uintptr_t)addr);
}
DI bf16x8 cat8(s16x4 lo, s16x4 hi) {
  s16x8 v = __builtin_shufflevector(lo, hi, 0, 1, 2, 3, 4, 5, 6, 7);
  return __builtin_bit_cast(bf16x8, v);
}
DI bf16x8 pack8(const f32x16& x, int s) {
  bf16x8 r;
#pragma unroll
  for (int j = 0; j < 8; ++j) r[j] = (__bf16)x[8 * s + j];
  return r;
}
DI float swap_max(float v) {
  auto rr = __builtin_amdgcn_permlane32_swap(__float_as_uint(v), __float_as_uint(v), false, false);
  return fmaxf(__uint_as_float(rr[0]), __uint_as_float(rr[1]));
}
DI float swap_sum(float v) {
  auto rr = __builtin_amdgcn_permlane32_swap(__float_as_uint(v), __float_as_uint(v), false, false);
  return __uint_as_float(rr[0]) + __uint_as_float(rr[1]);
}

constexpr int LDS_BYTES = 160 * 1024;
constexpr float RESC_THR = 8.0f;
#define KVSCHED 0
#define STAMP(i)
#define STAMP2(i)
#define SEG(k)

__global__ __launch_bounds__(512, 2) void attn_kernel(const __bf16* __restrict__ x, const __bf16* __restrict__ kc,
                                                      const __bf16* __restrict__ vc, const __bf16* __restrict__ wq,
                                                      const __bf16* __restrict__ wo, __bf16* __restrict__ obuf,
                                                      float* __restrict__ slabs, unsigned* __restrict__ cnt,
                                                      __bf16* __restrict__ out) {
  __shared__ __attribute__((aligned(1024))) char smem[LDS_BYTES];
  const int tid = threadIdx.x, lane = tid & 63;
  const int wid = __builtin_amdgcn_readfirstlane(tid >> 6);
  const int rg = wid & 3, kh = wid >> 2;
  const int r = lane & 31, h = lane >> 5;
  const int bh = blockIdx.x, b = bh >> 5, hd = bh & 31;
  const unsigned lds0 = (unsigned)(uintptr_t)smem;

  const char* Kg = (const char*)(kc + (size_t)bh * SK * DH);
  const char* Vg = (const char*)(vc + (size_t)bh * SK * DH);
  STAMP(0);
  const int dc = lane & 15;
  const char* Wg = (const char*)(wq + hd * DH);
  const char* Xg = (const char*)(x + (size_t)b * SQ * DM);
  auto dma_stage = [&](int c, int reg) {
    const unsigned base = lds0 + (unsigned)reg * 32768u + (unsigned)rg * 4096u;
#pragma unroll
    for (int p = 0; p < 4; ++p) {
      const int xr = 32 * rg + 8 * p + (lane >> 3), xc = lane & 7;
      glds16(Xg + c * 128 + min(xr, SQ - 1) * 1024 + ((xc ^ ((xr >> 1) & 7)) << 4), base + p * 1024);
    }
#pragma unroll
    for (int p = 0; p < 4; ++p) {
      const int wr = 16 * rg + 4 * p + (lane >> 4);
      glds16(Wg + (size_t)(c * 64 + wr) * 8192 + ((dc ^ ((wr & 3) << 2)) << 4), base + 16384 + p * 1024);
    }
  };
  u32x4 pre[3][8];
  auto kv_rows = [&](int t, int p, const char*& ks, const char*& vs) {
    const int row = 16 * rg + 4 * p + (lane >> 4);
    ks = Kg + (size_t)t * 16384 + row * 256 + ((dc ^ (row & 15)) << 4);
    vs = Vg + (size_t)t * 16384 + row * 256 + ((dc ^ ((row & 3) << 2)) << 4);
  };
  auto kv_dma4 = [&](int t) {
#pragma unroll
    for (int p = 0; p < 4; ++p) {
      const char *ks, *vs; kv_rows(t, p, ks, vs);
      const unsigned base = lds0 + (unsigned)t * 32768u + (unsigned)rg * 4096u + p * 1024;
      glds16nt(ks, base); glds16nt(vs, base + 16384);
    }
  };
  auto kv_reg4 = [&](int i) {
#pragma unroll
    for (int p = 0; p < 4; ++p) {
      const char *ks, *vs; kv_rows(2 + i, p, ks, vs);
      pre[i][p] = __builtin_nontemporal_load(reinterpret_cast<const u32x4*>(ks));
      pre[i][4 + p] = __builtin_nontemporal_load(reinterpret_cast<const u32x4*>(vs));
    }
  };
  unsigned tch[8] = {0, 0, 0, 0, 0, 0, 0, 0};
  if (kh == 0) {
    dma_stage(0, 2); dma_stage(1, 3); dma_stage(2, 4);
  } else {
    kv_dma4(0); kv_dma4(1);
  }
  auto put_pre = [&](int i, int reg) {
    char* dst = smem + reg * 32768 + rg * 4096 + lane * 16;
#pragma unroll
    for (int p = 0; p < 4; ++p) {
      *reinterpret_cast<u32x4*>(dst + p * 1024) = pre[i][p];
      *reinterpret_cast<u32x4*>(dst + 16384 + p * 1024) = pre[i][4 + p];
    }
  };

  const int tq_ = (lane & 15) >> 2, tp = lane & 3, tg = (lane >> 4) & 1;
  const int tpp = ((tp & 1) << 1) | (tp >> 1);
  f32x16 qa[2] = {};
  const int xrow = rg * 32 + r;
  auto qproj_chunk = [&](int reg) {
    const unsigned base = lds0 + (unsigned)reg * 32768u;
#pragma unroll
    for (int s = 0; s < 4; ++s) {
      const bf16x8 xf = *reinterpret_cast<const bf16x8*>(smem + reg * 32768 + xrow * 128 + (((2 * s + h) ^ ((xrow >> 1) & 7)) << 4));
#pragma unroll
      for (int dd = 0; dd < 2; ++dd) {
        const int dt = 2 * kh + dd;
        const int ch = 4 * dt + 2 * tg + (tpp >> 1);
        const int row0 = 16 * s + 8 * h + tq_;
        const unsigned a0 = base + 16384 + row0 * 256 + ((ch ^ (tq_ << 2)) << 4) + (tpp & 1) * 8;
        s16x4 lo = tr_read(a0), hi = tr_read(a0 + 1024);
        qa[dd] = MFMA32(cat8(lo, hi), xf, qa[dd]);
      }
    }
  };
  if (kh == 0) VMCNT(16);
  asm volatile("" :: "v"(tch[0]), "v"(tch[1]), "v"(tch[2]), "v"(tch[3]), "v"(tch[4]), "v"(tch[5]), "v"(tch[6]), "v"(tch[7]));
#define KV1 kv_reg4(0)
#define KV2
#define KV3 kv_reg4(1)
#define KV4
#define KV5 kv_reg4(2)
#define KV6
#define KV7
  BARRIER(); STAMP(1); qproj_chunk(2);
  if (kh == 0) VMCNT(8);
  BARRIER(); if (kh == 0) dma_stage(3, 2); else { KV1; } qproj_chunk(3);
  if (kh == 0) VMCNT(8);
  BARRIER(); if (kh == 0) dma_stage(4, 3); else { KV2; } qproj_chunk(4);
  if (kh == 0) VMCNT(8);
  BARRIER(); if (kh == 0) dma_stage(5, 4); else { KV3; } qproj_chunk(2);
  if (kh == 0) VMCNT(8);
  BARRIER(); if (kh == 0) dma_stage(6, 2); else { KV4; } qproj_chunk(3);
  if (kh == 0) VMCNT(8);
  BARRIER(); if (kh == 0) dma_stage(7, 3); else { KV5; } qproj_chunk(4);
  if (kh == 0) VMCNT(8);
  BARRIER(); if (kh == 1) { KV6; } qproj_chunk(2);
  if (kh == 0) VMCNT(0);
  BARRIER(); if (kh == 1) { KV7; } qproj_chunk(3);
  {
    char* qx = smem + 131072 + rg * 8192 + lane * 16;
#pragma unroll
    for (int dd = 0; dd < 2; ++dd)
#pragma unroll
      for (int s = 0; s < 2; ++s) *reinterpret_cast<bf16x8*>(qx + ((2 * kh + dd) * 2 + s) * 1024) = pack8(qa[dd], s);
  }
  BARRIER();
  STAMP(2);
  bf16x8 qf[8];
  {
    const char* qx = smem + 131072 + rg * 8192 + lane * 16;
#pragma unroll
    for (int f = 0; f < 8; ++f) qf[f] = *reinterpret_cast<const bf16x8*>(qx + f * 1024);
  }
  if (kh == 1) { put_pre(0, 2); put_pre(1, 3); VMCNT(0); }
  BARRIER();
  if (kh == 1) put_pre(2, 4);

  const int drow0 = 8 * wid + (lane >> 4), drow1 = drow0 + 4;
  const int koff0 = drow0 * 256 + ((dc ^ (drow0 & 15)) << 4), koff1 = drow1 * 256 + ((dc ^ (drow1 & 15)) << 4);
  const int voff0 = drow0 * 256 + ((dc ^ ((drow0 & 3) << 2)) << 4), voff1 = drow1 * 256 + ((dc ^ ((drow1 & 3) << 2)) << 4);
  auto dma_tile = [&](int t, int slot) {
    const unsigned base = lds0 + (unsigned)slot * 32768u + (unsigned)wid * 2048u;
    const char* ks = Kg + (size_t)t * 16384; const char* vs = Vg + (size_t)t * 16384;
    glds16nt(ks + koff0, base); glds16nt(ks + koff1, base + 1024);
    glds16nt(vs + voff0, base + 16384); glds16nt(vs + voff1, base + 16384 + 1024);
  };
  const int ks = hd >> 3, nt = hd & 7;
  const char* WOg = (const char*)(wo + (size_t)ks * 1024 * 512 + nt * 64);
  const int wbr = 8 * wid + (lane >> 3), wbc = lane & 7;
  const int wboff = wbr * 1024 + ((wbc ^ (((wbr >> 1) & 1) << 2)) << 4);
  auto dma_wo = [&](int c, int loc) {
    glds16(WOg + (size_t)c * 65536 + wboff, lds0 + 65536u + (unsigned)loc * 8192u + (unsigned)wid * 1024u);
  };
  f32x16 o[4] = {};
  const int krow = 32 * kh + r;
  const unsigned kaddr = krow * 256;
  const int ksw = krow & 15;
  const unsigned vaddr = 16384 + (32 * kh + 4 * h + tq_) * 256 + (tp & 1) * 8;
  auto rowmax = [&](const f32x16& sv) {
    float pm = sv[0];
#pragma unroll
    for (int i = 1; i < 16; ++i) pm = fmaxf(pm, sv[i]);
    return swap_max(pm) * SL2E;
  };
  auto load_k = [&](bf16x8* kf, int sl) {
#pragma unroll
    for (int s = 0; s < 8; ++s) kf[s] = *reinterpret_cast<const bf16x8*>(smem + sl * 32768 + kaddr + (((2 * s + h) ^ ksw) << 4));
  };
  auto load_v = [&](s16x4* vlo, s16x4* vhi, int sl) {
    const unsigned slot = lds0 + (unsigned)sl * 32768u;
#pragma unroll
    for (int dt = 0; dt < 4; ++dt)
#pragma unroll
      for (int s = 0; s < 2; ++s) {
        const unsigned a0 = slot + vaddr + s * 4096 + (((4 * dt + 2 * tg + (tp >> 1)) ^ (tq_ << 2)) << 4);
        vlo[2 * dt + s] = tr_read(a0); vhi[2 * dt + s] = tr_read(a0 + 2048);
      }
  };
  bf16x8 kf[8];
  s16x4 vlo[8], vhi[8];
  load_k(kf, 0);
  f32x16 scur = {};
#pragma unroll
  for (int s = 0; s < 8; ++s) scur = MFMA32(kf[s], qf[s], scur);
  load_k(kf, 1);
  load_v(vlo, vhi, 0);
  float m_run = rowmax(scur), l_run = 0.f;
  int cur = 0;
  STAMP(3);
  for (int t = 0; t < NTILE; ++t) {
    if (t < NTILE - 4) { VMCNT(8); } else if (t < NTILE - 2) { VMCNT(4); }
    SEG(0);
    BARRIER();
    SEG(1);
    const int n1 = cur == 4 ? 0 : cur + 1, n2 = n1 == 4 ? 0 : n1 + 1;
    f32x16 snext = {};
#pragma unroll
    for (int s = 0; s < 8; ++s) snext = MFMA32(kf[s], qf[s], snext);
    __builtin_amdgcn_sched_barrier(0);
    SEG(2);
    if (t + 5 < NTILE) dma_tile(t + 5, cur);
    else if (t >= 12 && t <= 14) {
#pragma unroll
      for (int i = 0; i < 4; ++i) { const int c = 4 * (t - 12) + i; if (c < 11) dma_wo(c, c); }
    }
    load_k(kf, n2);
    __builtin_amdgcn_sched_barrier(0);
    SEG(3);
    float ps = 0.f;
#pragma unroll
    for (int i = 0; i < 16; ++i) { scur[i] = __builtin_amdgcn_exp2f(fmaf(scur[i], SL2E, -m_run)); ps += scur[i]; }
    l_run += ps;
    const bf16x8 p0 = pack8(scur, 0), p1 = pack8(scur, 1);
    SEG(4);
#pragma unroll
    for (int dt = 0; dt < 4; ++dt) {
      o[dt] = MFMA32(cat8(vlo[2 * dt], vhi[2 * dt]), p0, o[dt]);
      o[dt] = MFMA32(cat8(vlo[2 * dt + 1], vhi[2 * dt + 1]), p1, o[dt]);
    }
    __builtin_amdgcn_sched_barrier(0);
    SEG(5);
    load_v(vlo, vhi, n1);
    const float pmax = rowmax(snext);
    if (t < NTILE - 1 && !__all(pmax <= m_run + RESC_THR)) {
      const float mn = fmaxf(m_run, pmax);
      const float alpha = __builtin_amdgcn_exp2f(m_run - mn);
      m_run = mn; l_run *= alpha;
#pragma unroll
      for (int d = 0; d < 4; ++d)
#pragma unroll
        for (int i = 0; i < 16; ++i) o[d][i] *= alpha;
    }
    scur = snext;
    cur = n1;
    SEG(6);
  }
  l_run = swap_sum(l_run);
  BARRIER();
  STAMP(19);
  {
    float* xo = reinterpret_cast<float*>(smem) + wid * 2048;
    float* xml = reinterpret_cast<float*>(smem + 155648) + wid * 128;
#pragma unroll
    for (int dd = 0; dd < 2; ++dd)
#pragma unroll
      for (int g = 0; g < 4; ++g) {
        f32x4 v;
#pragma unroll
        for (int j = 0; j < 4; ++j) v[j] = kh ? o[dd][4 * g + j] : o[2 + dd][4 * g + j];
        *reinterpret_cast<f32x4*>(xo + ((dd * 4 + g) * 64 + lane) * 4) = v;
      }
    xml[lane] = m_run; xml[64 + lane] = l_run;
  }
  BARRIER();
  bf16x4 ow[8];
  {
    const float* xo = reinterpret_cast<const float*>(smem) + (wid ^ 4) * 2048;
    const float* xml = reinterpret_cast<const float*>(smem + 155648) + (wid ^ 4) * 128;
    const float m2 = xml[lane], l2 = xml[64 + lane];
    const float mn = fmaxf(m_run, m2);
    const float a1 = __builtin_amdgcn_exp2f(m_run - mn), a2 = __builtin_amdgcn_exp2f(m2 - mn);
    const float inv = 1.f / (l_run * a1 + l2 * a2);
    const float s1 = a1 * inv, s2 = a2 * inv;
#pragma unroll
    for (int dd = 0; dd < 2; ++dd)
#pragma unroll
      for (int g = 0; g < 4; ++g) {
        const f32x4 v = *reinterpret_cast<const f32x4*>(xo + ((dd * 4 + g) * 64 + lane) * 4);
#pragma unroll
        for (int j = 0; j < 4; ++j) ow[dd * 4 + g][j] = (__bf16)((kh ? o[2 + dd][4 * g + j] : o[dd][4 * g + j]) * s1 + v[j] * s2);
      }
  }
  BARRIER();
  {
    char* ot = smem + xrow * 256;
#pragma unroll
    for (int dd = 0; dd < 2; ++dd)
#pragma unroll
      for (int g = 0; g < 4; ++g)
        *reinterpret_cast<bf16x4*>(ot + (((4 * (2 * kh + dd) + g) ^ (xrow & 15)) << 4) + h * 8) = ow[dd * 4 + g];
  }
  BARRIER();
  {
    const __amdgpu_buffer_rsrc_t ro = __builtin_amdgcn_make_buffer_rsrc(obuf + (size_t)b * SQ * (NH * DH), 0, SQ * NH * DH * 2, 0x00020000);
#pragma unroll
    for (int i = 0; i < 4; ++i) {
      const int idx = tid + i * 512;
      if (idx < SQ * 16) {
        const int row = idx >> 4, ch = idx & 15;
        const u32x4 v = *reinterpret_cast<const u32x4*>(smem + row * 256 + ((ch ^ (row & 15)) << 4));
        __builtin_amdgcn_raw_buffer_store_b128(v, ro, row * 8192 + hd * 256 + ch * 16, 0, 16);
      }
    }
  }
  VMCNT(0);
  BARRIER();
  STAMP(20);
  unsigned* ready = cnt + 64 + b * 4 + ks;
  if (tid == 0) {
    __hip_atomic_fetch_add(ready, 1u, __ATOMIC_RELAXED, __HIP_MEMORY_SCOPE_AGENT);
    unsigned spins = 0;
    while (__hip_atomic_load(ready, __ATOMIC_RELAXED, __HIP_MEMORY_SCOPE_AGENT) < 8u && ++spins < (1u << 22)) __builtin_amdgcn_s_sleep(2);
  }
  BARRIER();
  STAMP2(0);

  const char* Ag = (const char*)(obuf + (size_t)b * SQ * 4096 + ks * 1024);
  const int ar0 = 16 * wid + (lane >> 3), ar1 = ar0 + 8;
  const int aoff0 = min(ar0, SQ - 1) * 8192 + ((wbc ^ ((ar0 >> 1) & 7)) << 4);
  const int aoff1 = min(ar1, SQ - 1) * 8192 + ((wbc ^ ((ar1 >> 1) & 7)) << 4);
  auto dma_o = [&](int c) {
    const unsigned base = lds0 + (unsigned)(c & 3) * 16384u + (unsigned)wid * 2048u;
    glds16sc1(Ag + c * 128 + aoff0, base); glds16sc1(Ag + c * 128 + aoff1, base + 1024);
  };
  dma_o(0); dma_o(1); dma_o(2); dma_o(3); dma_wo(11, 11);
  f32x16 acc = {};
  auto gemm_step = [&](int c) {
    const char* sb = smem + (c & 3) * 16384;
    const unsigned bb = lds0 + 65536u + (unsigned)(c < 12 ? c : c - 12) * 8192u;
#pragma unroll
    for (int s = 0; s < 4; ++s) {
      const bf16x8 af = *reinterpret_cast<const bf16x8*>(sb + xrow * 128 + (((2 * s + h) ^ ((xrow >> 1) & 7)) << 4));
      const int row0 = 16 * s + 8 * h + tq_;
      const unsigned a0 = bb + row0 * 128 + (((4 * kh + 2 * tg + (tp >> 1)) ^ (((row0 >> 1) & 1) << 2)) << 4) + (tp & 1) * 8;
      s16x4 lo = tr_read(a0), hi = tr_read(a0 + 512);
      acc = MFMA32(cat8(lo, hi), af, acc);
    }
  };
#define GSTEP(c, n) do { VMCNT(n); BARRIER(); if ((c) >= 1 && (c) + 3 < 16) dma_o((c) + 3); if ((c) >= 1 && (c) <= 4) dma_wo((c) + 11, (c) - 1); gemm_step(c); } while (0)
  GSTEP(0, 7); GSTEP(1, 5); GSTEP(2, 6); GSTEP(3, 7); GSTEP(4, 7); GSTEP(5, 7); GSTEP(6, 6); GSTEP(7, 5);
  GSTEP(8, 4); GSTEP(9, 4); GSTEP(10, 4); GSTEP(11, 4); GSTEP(12, 4); GSTEP(13, 4); GSTEP(14, 2); GSTEP(15, 0);
#undef GSTEP
  STAMP2(1);
  BARRIER();
#pragma unroll
  for (int g = 0; g < 4; ++g) {
    f32x4 v = {acc[4 * g], acc[4 * g + 1], acc[4 * g + 2], acc[4 * g + 3]};
    *reinterpret_cast<f32x4*>(smem + xrow * 256 + (((8 * kh + 2 * g + h) ^ (xrow & 15)) << 4)) = v;
  }
  BARRIER();
  const int tile = b * 8 + nt;
  const __amdgpu_buffer_rsrc_t rs = __builtin_amdgcn_make_buffer_rsrc(slabs + (size_t)tile * 4 * (128 * 64), 0, 4 * 128 * 64 * 4, 0x00020000);
#pragma unroll
  for (int i = 0; i < 4; ++i) {
    const int idx = tid + i * 512;
    if (idx < SQ * 16) {
      const int row = idx >> 4, ch = idx & 15;
      const u32x4 v = *reinterpret_cast<const u32x4*>(smem + row * 256 + ((ch ^ (row & 15)) << 4));
      __builtin_amdgcn_raw_buffer_store_b128(v, rs, ks * (128 * 64 * 4) + row * 256 + ch * 16, 0, 16);
    }
  }
  VMCNT(0);
  BARRIER();
  STAMP2(2);
  unsigned* flag = reinterpret_cast<unsigned*>(smem + 155648);
  if (tid == 0) *flag = __hip_atomic_fetch_add(cnt + tile, 1u, __ATOMIC_RELAXED, __HIP_MEMORY_SCOPE_AGENT);
  BARRIER();
  STAMP2(3);
  if (*flag != 3) { STAMP2(4); return; }
  for (int idx = tid; idx < SQ * 16; idx += 512) {
    const int row = idx >> 4, c4 = (idx & 15) * 4;
    f32x4 v = __builtin_bit_cast(f32x4, __builtin_amdgcn_raw_buffer_load_b128(rs, (row * 64 + c4) * 4, 0, 16));
#pragma unroll
    for (int k = 1; k < 4; ++k)
      v += __builtin_bit_cast(f32x4, __builtin_amdgcn_raw_buffer_load_b128(rs, (k * (128 * 64) + row * 64 + c4) * 4, 0, 16));
    bf16x4 w;
#pragma unroll
    for (int j = 0; j < 4; ++j) w[j] = (__bf16)v[j];
    *reinterpret_cast<bf16x4*>(out + ((size_t)b * SQ + row) * 512 + nt * 64 + c4) = w;
  }
  STAMP2(4);
}

extern "C" void kernel_launch(void* const* d_in, const int* in_sizes, int n_in, void* d_out, int out_size, void* d_ws,
                              size_t ws_size, hipStream_t stream) {
  const __bf16* x = (const __bf16*)d_in[0];
  const __bf16* kc = (const __bf16*)d_in[1];
  const __bf16* vc = (const __bf16*)d_in[2];
  const __bf16* wq = (const __bf16*)d_in[3];
  const __bf16* wo = (const __bf16*)d_in[4];
  unsigned* cnt = (unsigned*)d_ws;
  __bf16* obuf = (__bf16*)((char*)d_ws + 4096);
  float* slabs = (float*)((char*)d_ws + (16u << 20));
  hipMemsetAsync(cnt, 0, 512, stream);
  attn_kernel<<<NB * NH, 512, 0, stream>>>(x, kc, vc, wq, wo, obuf, slabs, cnt, (__bf16*)d_out);
}
```

```cpp
#include <hip/hip_runtime.h>
#include <hip/hip_bf16.h>
#include <stdint.h>

constexpr int NB = 8, SQ = 112, DM = 512, NH = 32, SK = 1024, DH = 128;
constexpr int NTILE = SK / 64;
constexpr float SL2E = 0.088388347648318447f * 1.4426950408889634f;

using bf16x8 = __attribute__((ext_vector_type(8))) __bf16;
using bf16x4 = __attribute__((ext_vector_type(4))) __bf16;
using s16x4 = __attribute__((ext_vector_type(4))) short;
using s16x8 = __attribute__((ext_vector_type(8))) short;
using f32x16 = __attribute__((ext_vector_type(16))) float;
using f32x4 = __attribute__((ext_vector_type(4))) float;
using u32x2 = __attribute__((ext_vector_type(2))) unsigned;
using u32x4 = __attribute__((ext_vector_type(4))) unsigned;
typedef __attribute__((address_space(3))) s16x4 lds_s16x4;

#define DI __device__ __forceinline__
#define MFMA32(a, b, c) __builtin_amdgcn_mfma_f32_32x32x16_bf16((a), (b), (c), 0, 0, 0)
#define CFENCE() asm volatile("" ::: "memory")
#define BARRIER() do { asm volatile("s_waitcnt lgkmcnt(0)" ::: "memory"); __builtin_amdgcn_s_barrier(); CFENCE(); } while (0)
#define VMCNT(n) asm volatile("s_waitcnt vmcnt(" #n ")" ::: "memory")

DI void glds16(const void* gsrc, unsigned lds_dst) {
  unsigned keep;
  asm volatile("s_mov_b32 %0, m0\n\ts_mov_b32 m0, %2\n\ts_nop 0\n\tglobal_load_lds_dwordx4 %1, off\n\ts_mov_b32 m0, %0"
               : "=&s"(keep) : "v"(gsrc), "s"(lds_dst) : "memory");
}
DI void glds16nt(const void* gsrc, unsigned lds_dst) {
  unsigned keep;
  asm volatile("s_mov_b32 %0, m0\n\ts_mov_b32 m0, %2\n\ts_nop 0\n\tglobal_load_lds_dwordx4 %1, off nt\n\ts_mov_b32 m0, %0"
               : "=&s"(keep) : "v"(gsrc), "s"(lds_dst) : "memory");
}
DI void glds16sc1(const void* gsrc, unsigned lds_dst) {
  unsigned keep;
  asm volatile("s_mov_b32 %0, m0\n\ts_mov_b32 m0, %2\n\ts_nop 0\n\tglobal_load_lds_dwordx4 %1, off sc1\n\ts_mov_b32 m0, %0"
               : "=&s"(keep) : "v"(gsrc), "s"(lds_dst) : "memory");
}
DI s16x4 tr_read(unsigned addr) {
  return __builtin_amdgcn_ds_read_tr16_b64_v4i16((lds_s16x4*)(uintptr_t)addr);
}
DI bf16x8 cat8(s16x4 lo, s16x4 hi) {
  s16x8 v = __builtin_shufflevector(lo, hi, 0, 1, 2, 3, 4, 5, 6, 7);
  return __builtin_bit_cast(bf16x8, v);
}
DI bf16x8 pack8(const f32x16& x, int s) {
  bf16x8 r;
#pragma unroll
  for (int j = 0; j < 8; ++j) r[j] = (__bf16)x[8 * s + j];
  return r;
}
DI float swap_max(float v) {
  auto rr = __builtin_amdgcn_permlane32_swap(__float_as_uint(v), __float_as_uint(v), false, false);
  return fmaxf(__uint_as_float(rr[0]), __uint_as_float(rr[1]));
}
DI float swap_sum(float v) {
  auto rr = __builtin_amdgcn_permlane32_swap(__float_as_uint(v), __float_as_uint(v), false, false);
  return __uint_as_float(rr[0]) + __uint_as_float(rr[1]);
}

constexpr int LDS_BYTES = 160 * 1024;
constexpr float RESC_THR = 8.0f;
#define KVSCHED 5
#define STAMP(i)
#define STAMP2(i)
#define SEG(k)

__global__ __launch_bounds__(512, 2) void attn_kernel(const __bf16* __restrict__ x, const __bf16* __restrict__ kc,
                                                      const __bf16* __restrict__ vc, const __bf16* __restrict__ wq,
                                                      const __bf16* __restrict__ wo, __bf16* __restrict__ obuf,
                                                      float* __restrict__ slabs, unsigned* __restrict__ cnt,
                                                      __bf16* __restrict__ out) {
  __shared__ __attribute__((aligned(1024))) char smem[LDS_BYTES];
  const int tid = threadIdx.x, lane = tid & 63;
  const int wid = __builtin_amdgcn_readfirstlane(tid >> 6);
  const int rg = wid & 3, kh = wid >> 2;
  const int r = lane & 31, h = lane >> 5;
  const int bh = blockIdx.x, b = bh >> 5, hd = bh & 31;
  const unsigned lds0 = (unsigned)(uintptr_t)smem;

  const char* Kg = (const char*)(kc + (size_t)bh * SK * DH);
  const char* Vg = (const char*)(vc + (size_t)bh * SK * DH);
  STAMP(0);
  const int dc = lane & 15;
  const char* Wg = (const char*)(wq + hd * DH);
  const char* Xg = (const char*)(x + (size_t)b * SQ * DM);
  auto dma_stage = [&](int c, int reg) {
    const unsigned base = lds0 + (unsigned)reg * 32768u + (unsigned)rg * 4096u;
#pragma unroll
    for (int p = 0; p < 4; ++p) {
      const int xr = 32 * rg + 8 * p + (lane >> 3), xc = lane & 7;
      glds16(Xg + c * 128 + min(xr, SQ - 1) * 1024 + ((xc ^ ((xr >> 1) & 7)) << 4), base + p * 1024);
    }
#pragma unroll
    for (int p = 0; p < 4; ++p) {
      const int wr = 16 * rg + 4 * p + (lane >> 4);
      glds16(Wg + (size_t)(c * 64 + wr) * 8192 + ((dc ^ ((wr & 3) << 2)) << 4), base + 16384 + p * 1024);
    }
  };
  u32x4 pre[3][8];
  auto kv_rows = [&](int t, int p, const char*& ks, const char*& vs) {
    const int row = 16 * rg + 4 * p + (lane >> 4);
    ks = Kg + (size_t)t * 16384 + row * 256 + ((dc ^ (row & 15)) << 4);
    vs = Vg + (size_t)t * 16384 + row * 256 + ((dc ^ ((row & 3) << 2)) << 4);
  };
  auto kv_dma4 = [&](int t) {
#pragma unroll
    for (int p = 0; p < 4; ++p) {
      const char *ks, *vs; kv_rows(t, p, ks, vs);
      const unsigned base = lds0 + (unsigned)t * 32768u + (unsigned)rg * 4096u + p * 1024;
      glds16nt(ks, base); glds16nt(vs, base + 16384);
    }
  };
  auto kv_reg4 = [&](int i) {
#pragma unroll
    for (int p = 0; p < 4; ++p) {
      const char *ks, *vs; kv_rows(2 + i, p, ks, vs);
      pre[i][p] = __builtin_nontemporal_load(reinterpret_cast<const u32x4*>(ks));
      pre[i][4 + p] = __builtin_nontemporal_load(reinterpret_cast<const u32x4*>(vs));
    }
  };
  unsigned tch[8] = {0, 0, 0, 0, 0, 0, 0, 0};
  if (kh == 0) {
    dma_stage(0, 2); dma_stage(1, 3); dma_stage(2, 4);
  } else {
  }
  auto put_pre = [&](int i, int reg) {
    char* dst = smem + reg * 32768 + rg * 4096 + lane * 16;
#pragma unroll
    for (int p = 0; p < 4; ++p) {
      *reinterpret_cast<u32x4*>(dst + p * 1024) = pre[i][p];
      *reinterpret_cast<u32x4*>(dst + 16384 + p * 1024) = pre[i][4 + p];
    }
  };

  const int tq_ = (lane & 15) >> 2, tp = lane & 3, tg = (lane >> 4) & 1;
  const int tpp = ((tp & 1) << 1) | (tp >> 1);
  f32x16 qa[2] = {};
  const int xrow = rg * 32 + r;
  auto qproj_chunk = [&](int reg) {
    const unsigned base = lds0 + (unsigned)reg * 32768u;
#pragma unroll
    for (int s = 0; s < 4; ++s) {
      const bf16x8 xf = *reinterpret_cast<const bf16x8*>(smem + reg * 32768 + xrow * 128 + (((2 * s + h) ^ ((xrow >> 1) & 7)) << 4));
#pragma unroll
      for (int dd = 0; dd < 2; ++dd) {
        const int dt = 2 * kh + dd;
        const int ch = 4 * dt + 2 * tg + (tpp >> 1);
        const int row0 = 16 * s + 8 * h + tq_;
        const unsigned a0 = base + 16384 + row0 * 256 + ((ch ^ (tq_ << 2)) << 4) + (tpp & 1) * 8;
        s16x4 lo = tr_read(a0), hi = tr_read(a0 + 1024);
        qa[dd] = MFMA32(cat8(lo, hi), xf, qa[dd]);
      }
    }
  };
  if (kh == 0) VMCNT(16);
  asm volatile("" :: "v"(tch[0]), "v"(tch[1]), "v"(tch[2]), "v"(tch[3]), "v"(tch[4]), "v"(tch[5]), "v"(tch[6]), "v"(tch[7]));
#define KV1
#define KV2 kv_dma4(0)
#define KV3
#define KV4 kv_dma4(1)
#define KV5
#define KV6
#define KV7
  BARRIER(); STAMP(1); qproj_chunk(2);
  if (kh == 0) VMCNT(8);
  BARRIER(); if (kh == 0) dma_stage(3, 2); else { KV1; } qproj_chunk(3);
  if (kh == 0) VMCNT(8);
  BARRIER(); if (kh == 0) dma_stage(4, 3); else { KV2; } qproj_chunk(4);
  if (kh == 0) VMCNT(8);
  BARRIER(); if (kh == 0) dma_stage(5, 4); else { KV3; } qproj_chunk(2);
  if (kh == 0) VMCNT(8);
  BARRIER(); if (kh == 0) dma_stage(6, 2); else { KV4; } qproj_chunk(3);
  if (kh == 0) VMCNT(8);
  BARRIER(); if (kh == 0) dma_stage(7, 3); else { KV5; } qproj_chunk(4);
  if (kh == 0) VMCNT(8);
  BARRIER(); if (kh == 1) { KV6; } qproj_chunk(2);
  if (kh == 0) VMCNT(0);
  BARRIER(); if (kh == 1) { KV7; } qproj_chunk(3);
  qa[0] *= SL2E; qa[1] *= SL2E;
  {
    char* qx = smem + 131072 + rg * 8192 + lane * 16;
#pragma unroll
    for (int dd = 0; dd < 2; ++dd)
#pragma unroll
      for (int s = 0; s < 2; ++s) *reinterpret_cast<bf16x8*>(qx + ((2 * kh + dd) * 2 + s) * 1024) = pack8(qa[dd], s);
  }
  BARRIER();
  STAMP(2);
  bf16x8 qf[8];
  {
    const char* qx = smem + 131072 + rg * 8192 + lane * 16;
#pragma unroll
    for (int f = 0; f < 8; ++f) qf[f] = *reinterpret_cast<const bf16x8*>(qx + f * 1024);
  }

  const int drow0 = 8 * wid + (lane >> 4), drow1 = drow0 + 4;
  const int koff0 = drow0 * 256 + ((dc ^ (drow0 & 15)) << 4), koff1 = drow1 * 256 + ((dc ^ (drow1 & 15)) << 4);
  const int voff0 = drow0 * 256 + ((dc ^ ((drow0 & 3) << 2)) << 4), voff1 = drow1 * 256 + ((dc ^ ((drow1 & 3) << 2)) << 4);
  auto dma_k = [&](int t, int slot) {
    const unsigned base = lds0 + (unsigned)slot * 32768u + (unsigned)wid * 2048u;
    const char* ks = Kg + (size_t)t * 16384;
    glds16nt(ks + koff0, base); glds16nt(ks + koff1, base + 1024);
  };
  auto dma_v = [&](int t, int slot) {
    const unsigned base = lds0 + (unsigned)slot * 32768u + (unsigned)wid * 2048u;
    const char* vs = Vg + (size_t)t * 16384;
    glds16nt(vs + voff0, base + 16384); glds16nt(vs + voff1, base + 16384 + 1024);
  };
  const int ob = hd & 7, oj = b * 4 + (hd >> 3), ks = oj >> 3, nt = oj & 7;
  const char* WOg = (const char*)(wo + (size_t)ks * 1024 * 512 + nt * 64);
  const int wbr = 8 * wid + (lane >> 3), wbc = lane & 7;
  const int wboff = wbr * 1024 + ((wbc ^ (((wbr >> 1) & 1) << 2)) << 4);
  auto dma_wo = [&](int c, int loc) {
    glds16(WOg + (size_t)c * 65536 + wboff, lds0 + 65536u + (unsigned)loc * 8192u + (unsigned)wid * 1024u);
  };
  f32x16 o[4] = {};
  const int krow = 32 * kh + r;
  const unsigned kaddr = krow * 256;
  const int ksw = krow & 15;
  const unsigned vaddr = 16384 + (32 * kh + 4 * h + tq_) * 256 + (tp & 1) * 8;
  auto rowmax = [&](const f32x16& sv) {
    float pm = sv[0];
#pragma unroll
    for (int i = 1; i < 16; ++i) pm = fmaxf(pm, sv[i]);
    return swap_max(pm);
  };
  auto load_k = [&](bf16x8* kf, int sl) {
#pragma unroll
    for (int s = 0; s < 8; ++s) kf[s] = *reinterpret_cast<const bf16x8*>(smem + sl * 32768 + kaddr + (((2 * s + h) ^ ksw) << 4));
  };
  auto load_v = [&](s16x4* vlo, s16x4* vhi, int sl, int d0) {
    const unsigned slot = lds0 + (unsigned)sl * 32768u;
#pragma unroll
    for (int dt = d0; dt < d0 + 2; ++dt)
#pragma unroll
      for (int s = 0; s < 2; ++s) {
        const unsigned a0 = slot + vaddr + s * 4096 + (((4 * dt + 2 * tg + (tp >> 1)) ^ (tq_ << 2)) << 4);
        vlo[2 * dt + s] = tr_read(a0); vhi[2 * dt + s] = tr_read(a0 + 2048);
      }
  };
  dma_k(2, 2); dma_v(2, 2); dma_k(3, 3); dma_v(3, 3);
  if (kh == 1) VMCNT(8);
  BARRIER();
  dma_k(4, 4); dma_v(4, 4);
  bf16x8 kf[8];
  s16x4 vlo[8], vhi[8];
  load_k(kf, 0);
  f32x16 scur = {};
#pragma unroll
  for (int s = 0; s < 8; ++s) scur = MFMA32(kf[s], qf[s], scur);
  load_k(kf, 1);
  load_v(vlo, vhi, 0, 0);
  float m_run = rowmax(scur), l_run = 0.f;
  scur -= m_run;
  f32x16 negm;
#pragma unroll
  for (int i = 0; i < 16; ++i) negm[i] = -m_run;
  int cur = 0;
  STAMP(3);
  for (int t = 0; t < NTILE; ++t) {
    if (t <= 10) { VMCNT(8); } else if (t == 11) { VMCNT(6); } else if (t == 12 || t == 14) { VMCNT(4); } else if (t == 13) { VMCNT(2); }
    SEG(0);
    BARRIER();
    SEG(1);
    const int n1 = cur == 4 ? 0 : cur + 1, n2 = n1 == 4 ? 0 : n1 + 1;
    f32x16 snext = MFMA32(kf[0], qf[0], negm);
#pragma unroll
    for (int s = 1; s < 8; ++s) snext = MFMA32(kf[s], qf[s], snext);
    __builtin_amdgcn_sched_barrier(0);
    SEG(2);
    if (t == 0) dma_k(5, 0);
    if (t + 6 < NTILE) dma_k(t + 6, n1);
    if (t >= 1 && t + 4 < NTILE) dma_v(t + 4, cur == 0 ? 4 : cur - 1);
    if (t >= 13) {
#pragma unroll
      for (int i = 0; i < 4; ++i) { const int c = 4 * (t - 13) + i; if (c < 11) dma_wo(c, c); }
    }
    load_v(vlo, vhi, cur, 2);
    __builtin_amdgcn_sched_barrier(0);
    SEG(3);
    float ps = 0.f;
#pragma unroll
    for (int i = 0; i < 16; ++i) { scur[i] = __builtin_amdgcn_exp2f(scur[i]); ps += scur[i]; }
    l_run += ps;
    const bf16x8 p0 = pack8(scur, 0), p1 = pack8(scur, 1);
    SEG(4);
#pragma unroll
    for (int dt = 0; dt < 4; ++dt) {
      o[dt] = MFMA32(cat8(vlo[2 * dt], vhi[2 * dt]), p0, o[dt]);
      o[dt] = MFMA32(cat8(vlo[2 * dt + 1], vhi[2 * dt + 1]), p1, o[dt]);
    }
    __builtin_amdgcn_sched_barrier(0);
    SEG(5);
    load_k(kf, n2);
    load_v(vlo, vhi, n1, 0);
    const float pmax = rowmax(snext);
    if (t < NTILE - 1 && !__all(pmax <= RESC_THR)) {
      const float dl = fmaxf(pmax, 0.f);
      const float alpha = __builtin_amdgcn_exp2f(-dl);
      m_run += dl; l_run *= alpha;
#pragma unroll
      for (int d = 0; d < 4; ++d)
#pragma unroll
        for (int i = 0; i < 16; ++i) o[d][i] *= alpha;
      snext -= dl;
#pragma unroll
      for (int i = 0; i < 16; ++i) negm[i] = -m_run;
    }
    scur = snext;
    cur = n1;
    SEG(6);
  }
  l_run = swap_sum(l_run);
  BARRIER();
  STAMP(19);
  {
    float* xo = reinterpret_cast<float*>(smem) + wid * 2048;
    float* xml = reinterpret_cast<float*>(smem + 155648) + wid * 128;
#pragma unroll
    for (int dd = 0; dd < 2; ++dd)
#pragma unroll
      for (int g = 0; g < 4; ++g) {
        f32x4 v;
#pragma unroll
        for (int j = 0; j < 4; ++j) v[j] = kh ? o[dd][4 * g + j] : o[2 + dd][4 * g + j];
        *reinterpret_cast<f32x4*>(xo + ((dd * 4 + g) * 64 + lane) * 4) = v;
      }
    xml[lane] = m_run; xml[64 + lane] = l_run;
  }
  BARRIER();
  bf16x4 ow[8];
  {
    const float* xo = reinterpret_cast<const float*>(smem) + (wid ^ 4) * 2048;
    const float* xml = reinterpret_cast<const float*>(smem + 155648) + (wid ^ 4) * 128;
    const float m2 = xml[lane], l2 = xml[64 + lane];
    const float mn = fmaxf(m_run, m2);
    const float a1 = __builtin_amdgcn_exp2f(m_run - mn), a2 = __builtin_amdgcn_exp2f(m2 - mn);
    const float inv = 1.f / (l_run * a1 + l2 * a2);
    const float s1 = a1 * inv, s2 = a2 * inv;
#pragma unroll
    for (int dd = 0; dd < 2; ++dd)
#pragma unroll
      for (int g = 0; g < 4; ++g) {
        const f32x4 v = *reinterpret_cast<const f32x4*>(xo + ((dd * 4 + g) * 64 + lane) * 4);
#pragma unroll
        for (int j = 0; j < 4; ++j) ow[dd * 4 + g][j] = (__bf16)((kh ? o[2 + dd][4 * g + j] : o[dd][4 * g + j]) * s1 + v[j] * s2);
      }
  }
  BARRIER();
  {
    char* ot = smem + xrow * 256;
#pragma unroll
    for (int dd = 0; dd < 2; ++dd)
#pragma unroll
      for (int g = 0; g < 4; ++g)
        *reinterpret_cast<bf16x4*>(ot + (((4 * (2 * kh + dd) + g) ^ (xrow & 15)) << 4) + h * 8) = ow[dd * 4 + g];
  }
  BARRIER();
  STAMP(4);
  {
    const __amdgpu_buffer_rsrc_t ro = __builtin_amdgcn_make_buffer_rsrc(obuf + (size_t)b * SQ * (NH * DH), 0, SQ * NH * DH * 2, 0x00020000);
#pragma unroll
    for (int i = 0; i < 4; ++i) {
      const int idx = tid + i * 512;
      if (idx < SQ * 16) {
        const int row = idx >> 4, ch = idx & 15;
        const u32x4 v = *reinterpret_cast<const u32x4*>(smem + row * 256 + ((ch ^ (row & 15)) << 4));
        __builtin_amdgcn_raw_buffer_store_b128(v, ro, (hd * SQ + row) * 256 + ch * 16, 0, 16);
      }
    }
  }
  VMCNT(0);
  BARRIER();
  STAMP(20);
  unsigned* ready = cnt + (64 + ob * 4 + ks) * 64;
  if (tid == 0) {
    __hip_atomic_fetch_add(cnt + (64 + b * 4 + (hd >> 3)) * 64, 1u, __ATOMIC_RELAXED, __HIP_MEMORY_SCOPE_AGENT);
    unsigned spins = 0;
    while (__hip_atomic_load(ready, __ATOMIC_RELAXED, __HIP_MEMORY_SCOPE_AGENT) < 8u && ++spins < (1u << 22)) __builtin_amdgcn_s_sleep(8);
  }
  BARRIER();
  STAMP2(0);

  const char* Ag = (const char*)(obuf + (size_t)ob * SQ * 4096 + (size_t)ks * 8 * SQ * DH);
  const int ar0 = 16 * wid + (lane >> 3), ar1 = ar0 + 8;
  const int aoff0 = min(ar0, SQ - 1) * 256 + ((wbc ^ ((ar0 >> 1) & 7)) << 4);
  const int aoff1 = min(ar1, SQ - 1) * 256 + ((wbc ^ ((ar1 >> 1) & 7)) << 4);
  auto dma_o = [&](int c) {
    const unsigned base = lds0 + (unsigned)(c & 3) * 16384u + (unsigned)wid * 2048u;
    glds16sc1(Ag + (c >> 1) * (SQ * 256) + (c & 1) * 128 + aoff0, base); glds16sc1(Ag + (c >> 1) * (SQ * 256) + (c & 1) * 128 + aoff1, base + 1024);
  };
  dma_o(0); dma_o(1); dma_o(2); dma_o(3); dma_wo(11, 11);
  f32x16 acc = {};
  auto gemm_step = [&](int c) {
    const char* sb = smem + (c & 3) * 16384;
    const unsigned bb = lds0 + 65536u + (unsigned)(c < 12 ? c : c - 12) * 8192u;
#pragma unroll
    for (int s = 0; s < 4; ++s) {
      const bf16x8 af = *reinterpret_cast<const bf16x8*>(sb + xrow * 128 + (((2 * s + h) ^ ((xrow >> 1) & 7)) << 4));
      const int row0 = 16 * s + 8 * h + tq_;
      const unsigned a0 = bb + row0 * 128 + (((4 * kh + 2 * tg + (tp >> 1)) ^ (((row0 >> 1) & 1) << 2)) << 4) + (tp & 1) * 8;
      s16x4 lo = tr_read(a0), hi = tr_read(a0 + 512);
      acc = MFMA32(cat8(lo, hi), af, acc);
    }
  };
#define GSTEP(c, n) do { VMCNT(n); BARRIER(); if ((c) >= 1 && (c) + 3 < 16) dma_o((c) + 3); if ((c) >= 1 && (c) <= 4) dma_wo((c) + 11, (c) - 1); gemm_step(c); } while (0)
  GSTEP(0, 7); GSTEP(1, 5); GSTEP(2, 6); GSTEP(3, 7); GSTEP(4, 7); GSTEP(5, 7); GSTEP(6, 6); GSTEP(7, 5);
  GSTEP(8, 4); GSTEP(9, 4); GSTEP(10, 4); GSTEP(11, 4); GSTEP(12, 4); GSTEP(13, 4); GSTEP(14, 2); GSTEP(15, 0);
#undef GSTEP
  STAMP2(1);
  BARRIER();
#pragma unroll
  for (int g = 0; g < 4; ++g) {
    f32x4 v = {acc[4 * g], acc[4 * g + 1], acc[4 * g + 2], acc[4 * g + 3]};
    *reinterpret_cast<f32x4*>(smem + xrow * 256 + (((8 * kh + 2 * g + h) ^ (xrow & 15)) << 4)) = v;
  }
  BARRIER();
  const int tile = ob * 8 + nt;
  const __amdgpu_buffer_rsrc_t rs = __builtin_amdgcn_make_buffer_rsrc(slabs + (size_t)tile * 4 * (128 * 64), 0, 4 * 128 * 64 * 4, 0x00020000);
#pragma unroll
  for (int i = 0; i < 4; ++i) {
    const int idx = tid + i * 512;
    if (idx < SQ * 16) {
      const int row = idx >> 4, ch = idx & 15;
      const u32x4 v = *reinterpret_cast<const u32x4*>(smem + row * 256 + ((ch ^ (row & 15)) << 4));
      __builtin_amdgcn_raw_buffer_store_b128(v, rs, ks * (128 * 64 * 4) + row * 256 + ch * 16, 0, 16);
    }
  }
  VMCNT(0);
  BARRIER();
  STAMP2(2);
  unsigned* flag = reinterpret_cast<unsigned*>(smem + 155648);
  if (tid == 0) *flag = __hip_atomic_fetch_add(cnt + tile * 64, 1u, __ATOMIC_RELAXED, __HIP_MEMORY_SCOPE_AGENT);
  BARRIER();
  STAMP2(3);
  if (*flag != 3) { STAMP2(4); return; }
  for (int idx = tid; idx < SQ * 16; idx += 512) {
    const int row = idx >> 4, c4 = (idx & 15) * 4;
    f32x4 v = __builtin_bit_cast(f32x4, __builtin_amdgcn_raw_buffer_load_b128(rs, (row * 64 + c4) * 4, 0, 16));
#pragma unroll
    for (int k = 1; k < 4; ++k)
      v += __builtin_bit_cast(f32x4, __builtin_amdgcn_raw_buffer_load_b128(rs, (k * (128 * 64) + row * 64 + c4) * 4, 0, 16));
    bf16x4 w;
#pragma unroll
    for (int j = 0; j < 4; ++j) w[j] = (__bf16)v[j];
    *reinterpret_cast<bf16x4*>(out + ((size_t)ob * SQ + row) * 512 + nt * 64 + c4) = w;
  }
  STAMP2(4);
}

extern "C" void kernel_launch(void* const* d_in, const int* in_sizes, int n_in, void* d_out, int out_size, void* d_ws,
                              size_t ws_size, hipStream_t stream) {
  const __bf16* x = (const __bf16*)d_in[0];
  const __bf16* kc = (const __bf16*)d_in[1];
  const __bf16* vc = (const __bf16*)d_in[2];
  const __bf16* wq = (const __bf16*)d_in[3];
  const __bf16* wo = (const __bf16*)d_in[4];
  unsigned* cnt = (unsigned*)d_ws;
  __bf16* obuf = (__bf16*)((char*)d_ws + 32768);
  float* slabs = (float*)((char*)d_ws + (16u << 20));
  hipMemsetAsync(cnt, 0, 96 * 256, stream);
  attn_kernel<<<NB * NH, 512, 0, stream>>>(x, kc, vc, wq, wo, obuf, slabs, cnt, (__bf16*)d_out);
}
```

```cpp
#include <hip/hip_runtime.h>
#include <hip/hip_bf16.h>
#include <stdint.h>

constexpr int NB = 8, SQ = 112, DM = 512, NH = 32, SK = 1024, DH = 128;
constexpr int NTILE = SK / 64;
constexpr float SL2E = 0.088388347648318447f * 1.4426950408889634f;

using bf16x8 = __attribute__((ext_vector_type(8))) __bf16;
using bf16x4 = __attribute__((ext_vector_type(4))) __bf16;
using s16x4 = __attribute__((ext_vector_type(4))) short;
using s16x8 = __attribute__((ext_vector_type(8))) short;
using f32x16 = __attribute__((ext_vector_type(16))) float;
using f32x4 = __attribute__((ext_vector_type(4))) float;
using u32x2 = __attribute__((ext_vector_type(2))) unsigned;
using u32x4 = __attribute__((ext_vector_type(4))) unsigned;
typedef __attribute__((address_space(3))) s16x4 lds_s16x4;

#define DI __device__ __forceinline__
#define MFMA32(a, b, c) __builtin_amdgcn_mfma_f32_32x32x16_bf16((a), (b), (c), 0, 0, 0)
#define CFENCE() asm volatile("" ::: "memory")
#define BARRIER() do { asm volatile("s_waitcnt lgkmcnt(0)" ::: "memory"); __builtin_amdgcn_s_barrier(); CFENCE(); } while (0)
#define VMCNT(n) asm volatile("s_waitcnt vmcnt(" #n ")" ::: "memory")

DI void glds16(const void* gsrc, unsigned lds_dst) {
  unsigned keep;
  asm volatile("s_mov_b32 %0, m0\n\ts_mov_b32 m0, %2\n\ts_nop 0\n\tglobal_load_lds_dwordx4 %1, off\n\ts_mov_b32 m0, %0"
               : "=&s"(keep) : "v"(gsrc), "s"(lds_dst) : "memory");
}
DI void glds16nt(const void* gsrc, unsigned lds_dst) {
  unsigned keep;
  asm volatile("s_mov_b32 %0, m0\n\ts_mov_b32 m0, %2\n\ts_nop 0\n\tglobal_load_lds_dwordx4 %1, off nt\n\ts_mov_b32 m0, %0"
               : "=&s"(keep) : "v"(gsrc), "s"(lds_dst) : "memory");
}
DI void glds16sc1(const void* gsrc, unsigned lds_dst) {
  unsigned keep;
  asm volatile("s_mov_b32 %0, m0\n\ts_mov_b32 m0, %2\n\ts_nop 0\n\tglobal_load_lds_dwordx4 %1, off sc1\n\ts_mov_b32 m0, %0"
               : "=&s"(keep) : "v"(gsrc), "s"(lds_dst) : "memory");
}
DI s16x4 tr_read(unsigned addr) {
  return __builtin_amdgcn_ds_read_tr16_b64_v4i16((lds_s16x4*)(uintptr_t)addr);
}
DI bf16x8 cat8(s16x4 lo, s16x4 hi) {
  s16x8 v = __builtin_shufflevector(lo, hi, 0, 1, 2, 3, 4, 5, 6, 7);
  return __builtin_bit_cast(bf16x8, v);
}
DI bf16x8 pack8(const f32x16& x, int s) {
  bf16x8 r;
#pragma unroll
  for (int j = 0; j < 8; ++j) r[j] = (__bf16)x[8 * s + j];
  return r;
}
DI float swap_max(float v) {
  auto rr = __builtin_amdgcn_permlane32_swap(__float_as_uint(v), __float_as_uint(v), false, false);
  return fmaxf(__uint_as_float(rr[0]), __uint_as_float(rr[1]));
}
DI float swap_sum(float v) {
  auto rr = __builtin_amdgcn_permlane32_swap(__float_as_uint(v), __float_as_uint(v), false, false);
  return __uint_as_float(rr[0]) + __uint_as_float(rr[1]);
}

constexpr int LDS_BYTES = 160 * 1024;
constexpr float RESC_THR = 8.0f;
#define KVSCHED 5
#define STAMP(i)
#define STAMP2(i)
#define SEG(k)

__global__ __launch_bounds__(512, 2) void attn_kernel(const __bf16* __restrict__ x, const __bf16* __restrict__ kc,
                                                      const __bf16* __restrict__ vc, const __bf16* __restrict__ wq,
                                                      const __bf16* __restrict__ wo, __bf16* __restrict__ obuf,
                                                      float* __restrict__ slabs, unsigned* __restrict__ cnt,
                                                      __bf16* __restrict__ out) {
  __shared__ __attribute__((aligned(1024))) char smem[LDS_BYTES];
  const int tid = threadIdx.x, lane = tid & 63;
  const int wid = __builtin_amdgcn_readfirstlane(tid >> 6);
  const int rg = wid & 3, kh = wid >> 2;
  const int r = lane & 31, h = lane >> 5;
  const int bh = blockIdx.x, b = bh >> 5, hd = bh & 31;
  const unsigned lds0 = (unsigned)(uintptr_t)smem;

  const char* Kg = (const char*)(kc + (size_t)bh * SK * DH);
  const char* Vg = (const char*)(vc + (size_t)bh * SK * DH);
  STAMP(0);
  const int dc = lane & 15;
  const int drow0 = 8 * wid + (lane >> 4), drow1 = drow0 + 4;
  const int koff0 = drow0 * 256 + ((dc ^ (drow0 & 15)) << 4), koff1 = drow1 * 256 + ((dc ^ (drow1 & 15)) << 4);
  const int voff0 = drow0 * 256 + ((dc ^ ((drow0 & 3) << 2)) << 4), voff1 = drow1 * 256 + ((dc ^ ((drow1 & 3) << 2)) << 4);
  auto dma_k = [&](int t, int slot) {
    const unsigned base = lds0 + (unsigned)slot * 32768u + (unsigned)wid * 2048u;
    const char* ks = Kg + (size_t)t * 16384;
    glds16nt(ks + koff0, base); glds16nt(ks + koff1, base + 1024);
  };
  auto dma_v = [&](int t, int slot) {
    const unsigned base = lds0 + (unsigned)slot * 32768u + (unsigned)wid * 2048u;
    const char* vs = Vg + (size_t)t * 16384;
    glds16nt(vs + voff0, base + 16384); glds16nt(vs + voff1, base + 16384 + 1024);
  };
  const char* Wg = (const char*)(wq + hd * DH);
  const char* Xg = (const char*)(x + (size_t)b * SQ * DM);
  const int xr0 = 16 * wid + (lane >> 3), xr1 = xr0 + 8, xc = lane & 7;
  const int xoff0 = min(xr0, SQ - 1) * 1024 + ((xc ^ ((xr0 >> 1) & 7)) << 4), xoff1 = min(xr1, SQ - 1) * 1024 + ((xc ^ ((xr1 >> 1) & 7)) << 4);
  const int woff0 = drow0 * 8192 + ((dc ^ ((drow0 & 3) << 2)) << 4), woff1 = drow1 * 8192 + ((dc ^ ((drow1 & 3) << 2)) << 4);
  auto dma_stage = [&](int c, int reg) {
    const unsigned base = lds0 + (unsigned)reg * 32768u + (unsigned)wid * 2048u;
    glds16(Xg + c * 128 + xoff0, base); glds16(Xg + c * 128 + xoff1, base + 1024);
    const char* ws_ = Wg + (size_t)c * 64 * 8192;
    glds16(ws_ + woff0, base + 16384); glds16(ws_ + woff1, base + 16384 + 1024);
  };
  dma_stage(0, 2); dma_stage(1, 3); dma_stage(2, 4);

  const int tq_ = (lane & 15) >> 2, tp = lane & 3, tg = (lane >> 4) & 1;
  const int tpp = ((tp & 1) << 1) | (tp >> 1);
  f32x16 qa[2] = {};
  const int xrow = rg * 32 + r;
  u32x4 pre[12];
  {
    bf16x8 xf[2][4]; s16x4 lo[2][8], hi[2][8];
    auto qload = [&](int reg, int set) {
      const unsigned base = lds0 + (unsigned)reg * 32768u;
#pragma unroll
      for (int s = 0; s < 4; ++s) {
        xf[set][s] = *reinterpret_cast<const bf16x8*>(smem + reg * 32768 + xrow * 128 + (((2 * s + h) ^ ((xrow >> 1) & 7)) << 4));
#pragma unroll
        for (int dd = 0; dd < 2; ++dd) {
          const int ch = 4 * (2 * kh + dd) + 2 * tg + (tpp >> 1);
          const int row0 = 16 * s + 8 * h + tq_;
          const unsigned a0 = base + 16384 + row0 * 256 + ((ch ^ (tq_ << 2)) << 4) + (tpp & 1) * 8;
          lo[set][2 * s + dd] = tr_read(a0); hi[set][2 * s + dd] = tr_read(a0 + 1024);
        }
      }
      __builtin_amdgcn_sched_barrier(0);
    };
    auto qmma = [&](int set) {
      __builtin_amdgcn_sched_barrier(0);
#pragma unroll
      for (int s = 0; s < 4; ++s) {
        qa[0] = MFMA32(cat8(lo[set][2 * s], hi[set][2 * s]), xf[set][s], qa[0]);
        qa[1] = MFMA32(cat8(lo[set][2 * s + 1], hi[set][2 * s + 1]), xf[set][s], qa[1]);
      }
    };
    auto kvp = [&](int i) {
      const unsigned base = lds0 + (unsigned)(i >> 2) * 32768u + (unsigned)wid * 2048u + ((i & 2) ? 16384u : 0u) + (unsigned)(i & 1) * 1024u;
      const char* g = ((i & 2) ? Vg : Kg) + (size_t)(i >> 2) * 16384;
      glds16nt(g + ((i & 2) ? ((i & 1) ? voff1 : voff0) : ((i & 1) ? koff1 : koff0)), base);
    };
    auto kvr = [&](int i) {
      const char* g = ((i & 2) ? Vg : Kg) + (size_t)(2 + (i >> 2)) * 16384;
      pre[i] = __builtin_nontemporal_load(reinterpret_cast<const u32x4*>(g + ((i & 2) ? ((i & 1) ? voff1 : voff0) : ((i & 1) ? koff1 : koff0))));
    };
    VMCNT(8); BARRIER(); STAMP(1); qload(2, 0); kvp(0); kvr(0); kvr(1);
    VMCNT(7); BARRIER(); STAMP(6); qload(3, 1); dma_stage(3, 2); kvp(1); kvr(2); kvr(3); qmma(0);
    VMCNT(10); BARRIER(); STAMP(7); qload(4, 0); dma_stage(4, 3); kvp(2); kvr(4); kvr(5); qmma(1);
    VMCNT(10); BARRIER(); STAMP(8); qload(2, 1); dma_stage(5, 4); kvp(3); kvr(6); kvr(7); qmma(0);
    VMCNT(10); BARRIER(); STAMP(9); qload(3, 0); dma_stage(6, 2); kvp(4); kvr(8); qmma(1);
    VMCNT(9); BARRIER(); STAMP(10); qload(4, 1); dma_stage(7, 3); kvp(5); kvr(9); qmma(0);
    VMCNT(8); BARRIER(); STAMP(11); qload(2, 0); kvp(6); kvr(10); qmma(1);
    VMCNT(4); BARRIER(); STAMP(12); qload(3, 1); kvp(7); kvr(11); qmma(0);
    qmma(1);
  }
  qa[0] *= SL2E; qa[1] *= SL2E;
  {
    char* qx = smem + 131072 + rg * 8192 + lane * 16;
#pragma unroll
    for (int dd = 0; dd < 2; ++dd)
#pragma unroll
      for (int s = 0; s < 2; ++s) *reinterpret_cast<bf16x8*>(qx + ((2 * kh + dd) * 2 + s) * 1024) = pack8(qa[dd], s);
  }
  BARRIER();
  STAMP(2);
  bf16x8 qf[8];
  {
    const char* qx = smem + 131072 + rg * 8192 + lane * 16;
#pragma unroll
    for (int f = 0; f < 8; ++f) qf[f] = *reinterpret_cast<const bf16x8*>(qx + f * 1024);
  }
  const int oj = b * 4 + (hd >> 3), ks = oj & 3, ob = ((hd & 7) >> 1) * 2 + ((oj >> 2) & 1), nt = (hd & 1) * 4 + (oj >> 3);
  const char* WOg = (const char*)(wo + (size_t)ks * 1024 * 512 + nt * 64);
  const int wbr = 8 * wid + (lane >> 3), wbc = lane & 7;
  const int wboff = wbr * 1024 + ((wbc ^ (((wbr >> 1) & 1) << 2)) << 4);
  auto dma_wo = [&](int c, int loc) {
    glds16(WOg + (size_t)c * 65536 + wboff, lds0 + 65536u + (unsigned)loc * 8192u + (unsigned)wid * 1024u);
  };
  f32x16 o[4] = {};
  const int krow = 32 * kh + r;
  const unsigned kaddr = krow * 256;
  const int ksw = krow & 15;
  const unsigned vaddr = 16384 + (32 * kh + 4 * h + tq_) * 256 + (tp & 1) * 8;
  auto rowmax = [&](const f32x16& sv) {
    float pm = sv[0];
#pragma unroll
    for (int i = 1; i < 16; ++i) pm = fmaxf(pm, sv[i]);
    return swap_max(pm);
  };
  auto load_k = [&](bf16x8* kf, int sl) {
#pragma unroll
    for (int s = 0; s < 8; ++s) kf[s] = *reinterpret_cast<const bf16x8*>(smem + sl * 32768 + kaddr + (((2 * s + h) ^ ksw) << 4));
  };
  auto load_v = [&](s16x4* vlo, s16x4* vhi, int sl, int d0) {
    const unsigned slot = lds0 + (unsigned)sl * 32768u;
#pragma unroll
    for (int dt = d0; dt < d0 + 2; ++dt)
#pragma unroll
      for (int s = 0; s < 2; ++s) {
        const unsigned a0 = slot + vaddr + s * 4096 + (((4 * dt + 2 * tg + (tp >> 1)) ^ (tq_ << 2)) << 4);
        vlo[2 * dt + s] = tr_read(a0); vhi[2 * dt + s] = tr_read(a0 + 2048);
      }
  };
  auto put_pre = [&](int tl) {
    char* dst = smem + (2 + tl) * 32768 + wid * 2048 + lane * 16;
#pragma unroll
    for (int i = 0; i < 4; ++i) *reinterpret_cast<u32x4*>(dst + (i >> 1) * 16384 + (i & 1) * 1024) = pre[4 * tl + i];
  };
  put_pre(0); put_pre(1);
  VMCNT(0);
  BARRIER();
  put_pre(2);
  bf16x8 kf[8];
  s16x4 vlo[8], vhi[8];
  load_k(kf, 0);
  f32x16 scur = {};
#pragma unroll
  for (int s = 0; s < 8; ++s) scur = MFMA32(kf[s], qf[s], scur);
  load_k(kf, 1);
  load_v(vlo, vhi, 0, 0);
  float m_run = rowmax(scur), l_run = 0.f;
  scur -= m_run;
  f32x16 negm;
#pragma unroll
  for (int i = 0; i < 16; ++i) negm[i] = -m_run;
  int cur = 0;
  STAMP(3);
  for (int t = 0; t < NTILE; ++t) {
    if (t <= 10) { VMCNT(8); } else if (t == 11) { VMCNT(6); } else if (t == 12 || t == 14) { VMCNT(4); } else if (t == 13) { VMCNT(2); }
    SEG(0);
    BARRIER();
    SEG(1);
    const int n1 = cur == 4 ? 0 : cur + 1, n2 = n1 == 4 ? 0 : n1 + 1;
    f32x16 snext = MFMA32(kf[0], qf[0], negm);
#pragma unroll
    for (int s = 1; s < 8; ++s) snext = MFMA32(kf[s], qf[s], snext);
    __builtin_amdgcn_sched_barrier(0);
    SEG(2);
    if (t == 0) dma_k(5, 0);
    if (t + 6 < NTILE) dma_k(t + 6, n1);
    if (t >= 1 && t + 4 < NTILE) dma_v(t + 4, cur == 0 ? 4 : cur - 1);
    if (t >= 13) {
#pragma unroll
      for (int i = 0; i < 4; ++i) { const int c = 4 * (t - 13) + i; if (c < 11) dma_wo(c, c); }
    }
    load_v(vlo, vhi, cur, 2);
    __builtin_amdgcn_sched_barrier(0);
    SEG(3);
    float ps = 0.f;
#pragma unroll
    for (int i = 0; i < 16; ++i) { scur[i] = __builtin_amdgcn_exp2f(scur[i]); ps += scur[i]; }
    l_run += ps;
    const bf16x8 p0 = pack8(scur, 0), p1 = pack8(scur, 1);
    SEG(4);
#pragma unroll
    for (int dt = 0; dt < 4; ++dt) {
      o[dt] = MFMA32(cat8(vlo[2 * dt], vhi[2 * dt]), p0, o[dt]);
      o[dt] = MFMA32(cat8(vlo[2 * dt + 1], vhi[2 * dt + 1]), p1, o[dt]);
    }
    __builtin_amdgcn_sched_barrier(0);
    SEG(5);
    load_k(kf, n2);
    load_v(vlo, vhi, n1, 0);
    const float pmax = rowmax(snext);
    if (t < NTILE - 1 && !__all(pmax <= RESC_THR)) {
      const float dl = fmaxf(pmax, 0.f);
      const float alpha = __builtin_amdgcn_exp2f(-dl);
      m_run += dl; l_run *= alpha;
#pragma unroll
      for (int d = 0; d < 4; ++d)
#pragma unroll
        for (int i = 0; i < 16; ++i) o[d][i] *= alpha;
      snext -= dl;
#pragma unroll
      for (int i = 0; i < 16; ++i) negm[i] = -m_run;
    }
    scur = snext;
    cur = n1;
    SEG(6);
  }
  l_run = swap_sum(l_run);
  BARRIER();
  STAMP(19);
  {
    float* xo = reinterpret_cast<float*>(smem) + wid * 2048;
    float* xml = reinterpret_cast<float*>(smem + 155648) + wid * 128;
#pragma unroll
    for (int dd = 0; dd < 2; ++dd)
#pragma unroll
      for (int g = 0; g < 4; ++g) {
        f32x4 v;
#pragma unroll
        for (int j = 0; j < 4; ++j) v[j] = kh ? o[dd][4 * g + j] : o[2 + dd][4 * g + j];
        *reinterpret_cast<f32x4*>(xo + ((dd * 4 + g) * 64 + lane) * 4) = v;
      }
    xml[lane] = m_run; xml[64 + lane] = l_run;
  }
  BARRIER();
  bf16x4 ow[8];
  {
    const float* xo = reinterpret_cast<const float*>(smem) + (wid ^ 4) * 2048;
    const float* xml = reinterpret_cast<const float*>(smem + 155648) + (wid ^ 4) * 128;
    const float m2 = xml[lane], l2 = xml[64 + lane];
    const float mn = fmaxf(m_run, m2);
    const float a1 = __builtin_amdgcn_exp2f(m_run - mn), a2 = __builtin_amdgcn_exp2f(m2 - mn);
    const float inv = 1.f / (l_run * a1 + l2 * a2);
    const float s1 = a1 * inv, s2 = a2 * inv;
#pragma unroll
    for (int dd = 0; dd < 2; ++dd)
#pragma unroll
      for (int g = 0; g < 4; ++g) {
        const f32x4 v = *reinterpret_cast<const f32x4*>(xo + ((dd * 4 + g) * 64 + lane) * 4);
#pragma unroll
        for (int j = 0; j < 4; ++j) ow[dd * 4 + g][j] = (__bf16)((kh ? o[2 + dd][4 * g + j] : o[dd][4 * g + j]) * s1 + v[j] * s2);
      }
  }
  BARRIER();
  {
    char* ot = smem + xrow * 256;
#pragma unroll
    for (int dd = 0; dd < 2; ++dd)
#pragma unroll
      for (int g = 0; g < 4; ++g)
        *reinterpret_cast<bf16x4*>(ot + (((4 * (2 * kh + dd) + g) ^ (xrow & 15)) << 4) + h * 8) = ow[dd * 4 + g];
  }
  BARRIER();
  STAMP(4);
  {
    const __amdgpu_buffer_rsrc_t ro = __builtin_amdgcn_make_buffer_rsrc(obuf + (size_t)b * SQ * (NH * DH), 0, SQ * NH * DH * 2, 0x00020000);
#pragma unroll
    for (int i = 0; i < 4; ++i) {
      const int idx = tid + i * 512;
      if (idx < SQ * 16) {
        const int row = idx >> 4, ch = idx & 15;
        const u32x4 v = *reinterpret_cast<const u32x4*>(smem + row * 256 + ((ch ^ (row & 15)) << 4));
        __builtin_amdgcn_raw_buffer_store_b128(v, ro, (hd * SQ + row) * 256 + ch * 16, 0, 16);
      }
    }
  }
  VMCNT(0);
  BARRIER();
  STAMP(20);
  unsigned* ready = cnt + (64 + ob * 4 + ks) * 64;
  if (tid == 0) {
    __hip_atomic_fetch_add(cnt + (64 + b * 4 + (hd >> 3)) * 64, 1u, __ATOMIC_RELAXED, __HIP_MEMORY_SCOPE_AGENT);
    unsigned spins = 0;
    while (__hip_atomic_load(ready, __ATOMIC_RELAXED, __HIP_MEMORY_SCOPE_AGENT) < 8u && ++spins < (1u << 22)) __builtin_amdgcn_s_sleep(8);
  }
  BARRIER();
  STAMP2(0);

  const char* Ag = (const char*)(obuf + (size_t)ob * SQ * 4096 + (size_t)ks * 8 * SQ * DH);
  const int ar0 = 16 * wid + (lane >> 3), ar1 = ar0 + 8;
  const int aoff0 = min(ar0, SQ - 1) * 256 + ((wbc ^ ((ar0 >> 1) & 7)) << 4);
  const int aoff1 = min(ar1, SQ - 1) * 256 + ((wbc ^ ((ar1 >> 1) & 7)) << 4);
  auto dma_o = [&](int c) {
    const unsigned base = lds0 + (unsigned)(c & 3) * 16384u + (unsigned)wid * 2048u;
    glds16sc1(Ag + (c >> 1) * (SQ * 256) + (c & 1) * 128 + aoff0, base); glds16sc1(Ag + (c >> 1) * (SQ * 256) + (c & 1) * 128 + aoff1, base + 1024);
  };
  dma_o(0); dma_o(1); dma_o(2); dma_o(3); dma_wo(11, 11);
  f32x16 acc = {};
  bf16x8 af[2][4]; s16x4 glo[2][4], ghi[2][4];
  auto gload = [&](int c) {
    const char* sb = smem + (c & 3) * 16384;
    const unsigned bb = lds0 + 65536u + (unsigned)(c < 12 ? c : c - 12) * 8192u;
#pragma unroll
    for (int s = 0; s < 4; ++s) {
      af[c & 1][s] = *reinterpret_cast<const bf16x8*>(sb + xrow * 128 + (((2 * s + h) ^ ((xrow >> 1) & 7)) << 4));
      const int row0 = 16 * s + 8 * h + tq_;
      const unsigned a0 = bb + row0 * 128 + (((4 * kh + 2 * tg + (tp >> 1)) ^ (((row0 >> 1) & 1) << 2)) << 4) + (tp & 1) * 8;
      glo[c & 1][s] = tr_read(a0); ghi[c & 1][s] = tr_read(a0 + 512);
    }
    __builtin_amdgcn_sched_barrier(0);
  };
  auto gmma = [&](int c) {
    __builtin_amdgcn_sched_barrier(0);
#pragma unroll
    for (int s = 0; s < 4; ++s) acc = MFMA32(cat8(glo[c & 1][s], ghi[c & 1][s]), af[c & 1][s], acc);
  };
#define GSTEP(c, n) do { VMCNT(n); BARRIER(); gload(c); if ((c) >= 1 && (c) + 3 < 16) dma_o((c) + 3); if ((c) >= 1 && (c) <= 4) dma_wo((c) + 11, (c) - 1); if ((c) >= 1) gmma((c) - 1); } while (0)
  GSTEP(0, 7); GSTEP(1, 5); GSTEP(2, 6); GSTEP(3, 7); GSTEP(4, 7); GSTEP(5, 7); GSTEP(6, 6); GSTEP(7, 5);
  GSTEP(8, 4); GSTEP(9, 4); GSTEP(10, 4); GSTEP(11, 4); GSTEP(12, 4); GSTEP(13, 4); GSTEP(14, 2); GSTEP(15, 0);
  gmma(15);
#undef GSTEP
  STAMP2(1);
  BARRIER();
#pragma unroll
  for (int g = 0; g < 4; ++g) {
    f32x4 v = {acc[4 * g], acc[4 * g + 1], acc[4 * g + 2], acc[4 * g + 3]};
    *reinterpret_cast<f32x4*>(smem + xrow * 256 + (((8 * kh + 2 * g + h) ^ (xrow & 15)) << 4)) = v;
  }
  BARRIER();
  const int tile = ob * 8 + nt;
  const __amdgpu_buffer_rsrc_t rs = __builtin_amdgcn_make_buffer_rsrc(slabs + (size_t)tile * 4 * (128 * 64), 0, 4 * 128 * 64 * 4, 0x00020000);
#pragma unroll
  for (int i = 0; i < 4; ++i) {
    const int idx = tid + i * 512;
    if (idx < SQ * 16) {
      const int row = idx >> 4, ch = idx & 15;
      const u32x4 v = *reinterpret_cast<const u32x4*>(smem + row * 256 + ((ch ^ (row & 15)) << 4));
      __builtin_amdgcn_raw_buffer_store_b128(v, rs, ks * (128 * 64 * 4) + row * 256 + ch * 16, 0, 16);
    }
  }
  VMCNT(0);
  BARRIER();
  STAMP2(2);
  unsigned* flag = reinterpret_cast<unsigned*>(smem + 155648);
  if (tid == 0) *flag = __hip_atomic_fetch_add(cnt + tile * 64, 1u, __ATOMIC_RELAXED, __HIP_MEMORY_SCOPE_AGENT);
  BARRIER();
  STAMP2(3);
  if (*flag != 3) { STAMP2(4); return; }
  for (int idx = tid; idx < SQ * 16; idx += 512) {
    const int row = idx >> 4, c4 = (idx & 15) * 4;
    f32x4 v = __builtin_bit_cast(f32x4, __builtin_amdgcn_raw_buffer_load_b128(rs, (row * 64 + c4) * 4, 0, 16));
#pragma unroll
    for (int k = 1; k < 4; ++k)
      v += __builtin_bit_cast(f32x4, __builtin_amdgcn_raw_buffer_load_b128(rs, (k * (128 * 64) + row * 64 + c4) * 4, 0, 16));
    bf16x4 w;
#pragma unroll
    for (int j = 0; j < 4; ++j) w[j] = (__bf16)v[j];
    *reinterpret_cast<bf16x4*>(out + ((size_t)ob * SQ + row) * 512 + nt * 64 + c4) = w;
  }
  STAMP2(4);
}

extern "C" void kernel_launch(void* const* d_in, const int* in_sizes, int n_in, void* d_out, int out_size, void* d_ws,
                              size_t ws_size, hipStream_t stream) {
  const __bf16* x = (const __bf16*)d_in[0];
  const __bf16* kc = (const __bf16*)d_in[1];
  const __bf16* vc = (const __bf16*)d_in[2];
  const __bf16* wq = (const __bf16*)d_in[3];
  const __bf16* wo = (const __bf16*)d_in[4];
  unsigned* cnt = (unsigned*)d_ws;
  __bf16* obuf = (__bf16*)((char*)d_ws + 32768);
  float* slabs = (float*)((char*)d_ws + (16u << 20));
  hipMemsetAsync(cnt, 0, 96 * 256, stream);
  attn_kernel<<<NB * NH, 512, 0, stream>>>(x, kc, vc, wq, wo, obuf, slabs, cnt, (__bf16*)d_out);
}
```

```cpp
#include <hip/hip_runtime.h>
#include <hip/hip_bf16.h>
#include <stdint.h>

constexpr int NB = 8, SQ = 112, DM = 512, NH = 32, SK = 1024, DH = 128;
constexpr int NTILE = SK / 64;
constexpr float SL2E = 0.088388347648318447f * 1.4426950408889634f;

using bf16x8 = __attribute__((ext_vector_type(8))) __bf16;
using bf16x4 = __attribute__((ext_vector_type(4))) __bf16;
using s16x4 = __attribute__((ext_vector_type(4))) short;
using s16x8 = __attribute__((ext_vector_type(8))) short;
using f32x16 = __attribute__((ext_vector_type(16))) float;
using f32x4 = __attribute__((ext_vector_type(4))) float;
using u32x2 = __attribute__((ext_vector_type(2))) unsigned;
using u32x4 = __attribute__((ext_vector_type(4))) unsigned;
typedef __attribute__((address_space(3))) s16x4 lds_s16x4;

#define DI __device__ __forceinline__
#define MFMA32(a, b, c) __builtin_amdgcn_mfma_f32_32x32x16_bf16((a), (b), (c), 0, 0, 0)
#define CFENCE() asm volatile("" ::: "memory")
#define BARRIER() do { asm volatile("s_waitcnt lgkmcnt(0)" ::: "memory"); __builtin_amdgcn_s_barrier(); CFENCE(); } while (0)
#define VMCNT(n) asm volatile("s_waitcnt vmcnt(" #n ")" ::: "memory")

DI void glds16(const void* gsrc, unsigned lds_dst) {
  unsigned keep;
  asm volatile("s_mov_b32 %0, m0\n\ts_mov_b32 m0, %2\n\ts_nop 0\n\tglobal_load_lds_dwordx4 %1, off\n\ts_mov_b32 m0, %0"
               : "=&s"(keep) : "v"(gsrc), "s"(lds_dst) : "memory");
}
DI void glds16nt(const void* gsrc, unsigned lds_dst) {
  unsigned keep;
  asm volatile("s_mov_b32 %0, m0\n\ts_mov_b32 m0, %2\n\ts_nop 0\n\tglobal_load_lds_dwordx4 %1, off nt\n\ts_mov_b32 m0, %0"
               : "=&s"(keep) : "v"(gsrc), "s"(lds_dst) : "memory");
}
DI void glds16sc1(const void* gsrc, unsigned lds_dst) {
  unsigned keep;
  asm volatile("s_mov_b32 %0, m0\n\ts_mov_b32 m0, %2\n\ts_nop 0\n\tglobal_load_lds_dwordx4 %1, off sc1\n\ts_mov_b32 m0, %0"
               : "=&s"(keep) : "v"(gsrc), "s"(lds_dst) : "memory");
}
DI s16x4 tr_read(unsigned addr) {
  return __builtin_amdgcn_ds_read_tr16_b64_v4i16((lds_s16x4*)(uintptr_t)addr);
}
DI bf16x8 cat8(s16x4 lo, s16x4 hi) {
  s16x8 v = __builtin_shufflevector(lo, hi, 0, 1, 2, 3, 4, 5, 6, 7);
  return __builtin_bit_cast(bf16x8, v);
}
DI bf16x8 pack8(const f32x16& x, int s) {
  bf16x8 r;
#pragma unroll
  for (int j = 0; j < 8; ++j) r[j] = (__bf16)x[8 * s + j];
  return r;
}
DI float swap_max(float v) {
  auto rr = __builtin_amdgcn_permlane32_swap(__float_as_uint(v), __float_as_uint(v), false, false);
  return fmaxf(__uint_as_float(rr[0]), __uint_as_float(rr[1]));
}
DI float swap_sum(float v) {
  auto rr = __builtin_amdgcn_permlane32_swap(__float_as_uint(v), __float_as_uint(v), false, false);
  return __uint_as_float(rr[0]) + __uint_as_float(rr[1]);
}

constexpr int LDS_BYTES = 160 * 1024;
constexpr float RESC_THR = 8.0f;

__global__ __launch_bounds__(512, 2) void attn_kernel(const __bf16* __restrict__ x, const __bf16* __restrict__ kc,
                                                      const __bf16* __restrict__ vc, const __bf16* __restrict__ wq,
                                                      const __bf16* __restrict__ wo, __bf16* __restrict__ obuf,
                                                      float* __restrict__ slabs, unsigned* __restrict__ cnt,
                                                      __bf16* __restrict__ out) {
  __shared__ __attribute__((aligned(1024))) char smem[LDS_BYTES];
  const int tid = threadIdx.x, lane = tid & 63;
  const int wid = __builtin_amdgcn_readfirstlane(tid >> 6);
  const int rg = wid & 3, kh = wid >> 2;
  const int r = lane & 31, h = lane >> 5;
  const int bh = blockIdx.x, b = bh >> 5, hd = bh & 31;
  const unsigned lds0 = (unsigned)(uintptr_t)smem;

  const char* Kg = (const char*)(kc + (size_t)bh * SK * DH);
  const char* Vg = (const char*)(vc + (size_t)bh * SK * DH);
  const int dc = lane & 15;
  const int drow0 = 8 * wid + (lane >> 4), drow1 = drow0 + 4;
  const int koff0 = drow0 * 256 + ((dc ^ (drow0 & 15)) << 4), koff1 = drow1 * 256 + ((dc ^ (drow1 & 15)) << 4);
  const int voff0 = drow0 * 256 + ((dc ^ ((drow0 & 3) << 2)) << 4), voff1 = drow1 * 256 + ((dc ^ ((drow1 & 3) << 2)) << 4);
  auto dma_k = [&](int t, int slot) {
    const unsigned base = lds0 + (unsigned)slot * 32768u + (unsigned)wid * 2048u;
    const char* ks = Kg + (size_t)t * 16384;
    glds16nt(ks + koff0, base); glds16nt(ks + koff1, base + 1024);
  };
  auto dma_v = [&](int t, int slot) {
    const unsigned base = lds0 + (unsigned)slot * 32768u + (unsigned)wid * 2048u;
    const char* vs = Vg + (size_t)t * 16384;
    glds16nt(vs + voff0, base + 16384); glds16nt(vs + voff1, base + 16384 + 1024);
  };
  const char* Wg = (const char*)(wq + hd * DH);
  const char* Xg = (const char*)(x + (size_t)b * SQ * DM);
  const int xr0 = 16 * wid + (lane >> 3), xr1 = xr0 + 8, xc = lane & 7;
  const int xoff0 = min(xr0, SQ - 1) * 1024 + ((xc ^ ((xr0 >> 1) & 7)) << 4), xoff1 = min(xr1, SQ - 1) * 1024 + ((xc ^ ((xr1 >> 1) & 7)) << 4);
  const int woff0 = drow0 * 8192 + ((dc ^ ((drow0 & 3) << 2)) << 4), woff1 = drow1 * 8192 + ((dc ^ ((drow1 & 3) << 2)) << 4);
  auto dma_stage = [&](int c, int reg) {
    const unsigned base = lds0 + (unsigned)reg * 32768u + (unsigned)wid * 2048u;
    glds16(Xg + c * 128 + xoff0, base); glds16(Xg + c * 128 + xoff1, base + 1024);
    const char* ws_ = Wg + (size_t)c * 64 * 8192;
    glds16(ws_ + woff0, base + 16384); glds16(ws_ + woff1, base + 16384 + 1024);
  };
  dma_stage(0, 2); dma_stage(1, 3); dma_stage(2, 4);

  const int tq_ = (lane & 15) >> 2, tp = lane & 3, tg = (lane >> 4) & 1;
  const int tpp = ((tp & 1) << 1) | (tp >> 1);
  f32x16 qa[2] = {};
  const int xrow = rg * 32 + r;
  u32x4 pre[12];
  {
    bf16x8 xf[2][4]; s16x4 lo[2][8], hi[2][8];
    auto qload = [&](int reg, int set) {
      const unsigned base = lds0 + (unsigned)reg * 32768u;
#pragma unroll
      for (int s = 0; s < 4; ++s) {
        xf[set][s] = *reinterpret_cast<const bf16x8*>(smem + reg * 32768 + xrow * 128 + (((2 * s + h) ^ ((xrow >> 1) & 7)) << 4));
#pragma unroll
        for (int dd = 0; dd < 2; ++dd) {
          const int ch = 4 * (2 * kh + dd) + 2 * tg + (tpp >> 1);
          const int row0 = 16 * s + 8 * h + tq_;
          const unsigned a0 = base + 16384 + row0 * 256 + ((ch ^ (tq_ << 2)) << 4) + (tpp & 1) * 8;
          lo[set][2 * s + dd] = tr_read(a0); hi[set][2 * s + dd] = tr_read(a0 + 1024);
        }
      }
      __builtin_amdgcn_sched_barrier(0);
    };
    auto qmma = [&](int set) {
      __builtin_amdgcn_sched_barrier(0);
#pragma unroll
      for (int s = 0; s < 4; ++s) {
        qa[0] = MFMA32(cat8(lo[set][2 * s], hi[set][2 * s]), xf[set][s], qa[0]);
        qa[1] = MFMA32(cat8(lo[set][2 * s + 1], hi[set][2 * s + 1]), xf[set][s], qa[1]);
      }
    };
    auto kvp = [&](int i) {
      const unsigned base = lds0 + (unsigned)(i >> 2) * 32768u + (unsigned)wid * 2048u + ((i & 2) ? 16384u : 0u) + (unsigned)(i & 1) * 1024u;
      const char* g = ((i & 2) ? Vg : Kg) + (size_t)(i >> 2) * 16384;
      glds16nt(g + ((i & 2) ? ((i & 1) ? voff1 : voff0) : ((i & 1) ? koff1 : koff0)), base);
    };
    auto kvr = [&](int i) {
      const char* g = ((i & 2) ? Vg : Kg) + (size_t)(2 + (i >> 2)) * 16384;
      pre[i] = __builtin_nontemporal_load(reinterpret_cast<const u32x4*>(g + ((i & 2) ? ((i & 1) ? voff1 : voff0) : ((i & 1) ? koff1 : koff0))));
    };
    VMCNT(8); BARRIER(); qload(2, 0); kvp(0); kvr(0); kvr(1);
    VMCNT(7); BARRIER(); qload(3, 1); dma_stage(3, 2); kvp(1); kvr(2); kvr(3); qmma(0);
    VMCNT(10); BARRIER(); qload(4, 0); dma_stage(4, 3); kvp(2); kvr(4); kvr(5); qmma(1);
    VMCNT(10); BARRIER(); qload(2, 1); dma_stage(5, 4); kvp(3); kvr(6); kvr(7); qmma(0);
    VMCNT(10); BARRIER(); qload(3, 0); dma_stage(6, 2); kvp(4); kvr(8); qmma(1);
    VMCNT(9); BARRIER(); qload(4, 1); dma_stage(7, 3); kvp(5); kvr(9); qmma(0);
    VMCNT(8); BARRIER(); qload(2, 0); kvp(6); kvr(10); qmma(1);
    VMCNT(4); BARRIER(); qload(3, 1); kvp(7); kvr(11); qmma(0);
    qmma(1);
  }
  qa[0] *= SL2E; qa[1] *= SL2E;
  {
    char* qx = smem + 131072 + rg * 8192 + lane * 16;
#pragma unroll
    for (int dd = 0; dd < 2; ++dd)
#pragma unroll
      for (int s = 0; s < 2; ++s) *reinterpret_cast<bf16x8*>(qx + ((2 * kh + dd) * 2 + s) * 1024) = pack8(qa[dd], s);
  }
  BARRIER();
  bf16x8 qf[8];
  {
    const char* qx = smem + 131072 + rg * 8192 + lane * 16;
#pragma unroll
    for (int f = 0; f < 8; ++f) qf[f] = *reinterpret_cast<const bf16x8*>(qx + f * 1024);
  }
  const int oj = b * 4 + (hd >> 3), ks = oj & 3, ob = ((hd & 7) >> 1) * 2 + ((oj >> 2) & 1), nt = (hd & 1) * 4 + (oj >> 3);
  const char* WOg = (const char*)(wo + (size_t)ks * 1024 * 512 + nt * 64);
  const int wbr = 8 * wid + (lane >> 3), wbc = lane & 7;
  const int wboff = wbr * 1024 + ((wbc ^ (((wbr >> 1) & 1) << 2)) << 4);
  auto dma_wo = [&](int c, int loc) {
    glds16(WOg + (size_t)c * 65536 + wboff, lds0 + 65536u + (unsigned)loc * 8192u + (unsigned)wid * 1024u);
  };
  f32x16 o[4] = {};
  const int krow = 32 * kh + r;
  const unsigned kaddr = krow * 256;
  const int ksw = krow & 15;
  const unsigned vaddr = 16384 + (32 * kh + 4 * h + tq_) * 256 + (tp & 1) * 8;
  auto rowmax = [&](const f32x16& sv) {
    float pm = sv[0];
#pragma unroll
    for (int i = 1; i < 16; ++i) pm = fmaxf(pm, sv[i]);
    return swap_max(pm);
  };
  auto load_k = [&](bf16x8* kf, int sl) {
#pragma unroll
    for (int s = 0; s < 8; ++s) kf[s] = *reinterpret_cast<const bf16x8*>(smem + sl * 32768 + kaddr + (((2 * s + h) ^ ksw) << 4));
  };
  auto load_v = [&](s16x4* vlo, s16x4* vhi, int sl, int d0) {
    const unsigned slot = lds0 + (unsigned)sl * 32768u;
#pragma unroll
    for (int dt = d0; dt < d0 + 2; ++dt)
#pragma unroll
      for (int s = 0; s < 2; ++s) {
        const unsigned a0 = slot + vaddr + s * 4096 + (((4 * dt + 2 * tg + (tp >> 1)) ^ (tq_ << 2)) << 4);
        vlo[2 * dt + s] = tr_read(a0); vhi[2 * dt + s] = tr_read(a0 + 2048);
      }
  };
  auto put_pre = [&](int tl) {
    char* dst = smem + (2 + tl) * 32768 + wid * 2048 + lane * 16;
#pragma unroll
    for (int i = 0; i < 4; ++i) *reinterpret_cast<u32x4*>(dst + (i >> 1) * 16384 + (i & 1) * 1024) = pre[4 * tl + i];
  };
  put_pre(0); put_pre(1);
  VMCNT(0);
  BARRIER();
  put_pre(2);
  bf16x8 kf[8];
  s16x4 vlo[8], vhi[8];
  load_k(kf, 0);
  f32x16 scur = {};
#pragma unroll
  for (int s = 0; s < 8; ++s) scur = MFMA32(kf[s], qf[s], scur);
  load_k(kf, 1);
  load_v(vlo, vhi, 0, 0);
  float m_run = rowmax(scur), l_run = 0.f;
  scur -= m_run;
  f32x16 negm;
#pragma unroll
  for (int i = 0; i < 16; ++i) negm[i] = -m_run;
  int cur = 0;
  for (int t = 0; t < NTILE; ++t) {
    if (t <= 10) { VMCNT(8); } else if (t == 11) { VMCNT(6); } else if (t == 12 || t == 14) { VMCNT(4); } else if (t == 13) { VMCNT(2); }
    BARRIER();
    const int n1 = cur == 4 ? 0 : cur + 1, n2 = n1 == 4 ? 0 : n1 + 1;
    f32x16 snext = MFMA32(kf[0], qf[0], negm);
#pragma unroll
    for (int s = 1; s < 8; ++s) snext = MFMA32(kf[s], qf[s], snext);
    __builtin_amdgcn_sched_barrier(0);
    if (t == 0) dma_k(5, 0);
    if (t + 6 < NTILE) dma_k(t + 6, n1);
    if (t >= 1 && t + 4 < NTILE) dma_v(t + 4, cur == 0 ? 4 : cur - 1);
    if (t >= 13) {
#pragma unroll
      for (int i = 0; i < 4; ++i) { const int c = 4 * (t - 13) + i; if (c < 11) dma_wo(c, c); }
    }
    load_v(vlo, vhi, cur, 2);
    __builtin_amdgcn_sched_barrier(0);
    float ps = 0.f;
#pragma unroll
    for (int i = 0; i < 16; ++i) { scur[i] = __builtin_amdgcn_exp2f(scur[i]); ps += scur[i]; }
    l_run += ps;
    const bf16x8 p0 = pack8(scur, 0), p1 = pack8(scur, 1);
#pragma unroll
    for (int dt = 0; dt < 4; ++dt) {
      o[dt] = MFMA32(cat8(vlo[2 * dt], vhi[2 * dt]), p0, o[dt]);
      o[dt] = MFMA32(cat8(vlo[2 * dt + 1], vhi[2 * dt + 1]), p1, o[dt]);
    }
    __builtin_amdgcn_sched_barrier(0);
    load_k(kf, n2);
    load_v(vlo, vhi, n1, 0);
    const float pmax = rowmax(snext);
    if (t < NTILE - 1 && !__all(pmax <= RESC_THR)) {
      const float dl = fmaxf(pmax, 0.f);
      const float alpha = __builtin_amdgcn_exp2f(-dl);
      m_run += dl; l_run *= alpha;
#pragma unroll
      for (int d = 0; d < 4; ++d)
#pragma unroll
        for (int i = 0; i < 16; ++i) o[d][i] *= alpha;
      snext -= dl;
#pragma unroll
      for (int i = 0; i < 16; ++i) negm[i] = -m_run;
    }
    scur = snext;
    cur = n1;
  }
  l_run = swap_sum(l_run);
  BARRIER();
  {
    float* xo = reinterpret_cast<float*>(smem) + wid * 2048;
    float* xml = reinterpret_cast<float*>(smem + 155648) + wid * 128;
#pragma unroll
    for (int dd = 0; dd < 2; ++dd)
#pragma unroll
      for (int g = 0; g < 4; ++g) {
        f32x4 v;
#pragma unroll
        for (int j = 0; j < 4; ++j) v[j] = kh ? o[dd][4 * g + j] : o[2 + dd][4 * g + j];
        *reinterpret_cast<f32x4*>(xo + ((dd * 4 + g) * 64 + lane) * 4) = v;
      }
    xml[lane] = m_run; xml[64 + lane] = l_run;
  }
  BARRIER();
  bf16x4 ow[8];
  {
    const float* xo = reinterpret_cast<const float*>(smem) + (wid ^ 4) * 2048;
    const float* xml = reinterpret_cast<const float*>(smem + 155648) + (wid ^ 4) * 128;
    const float m2 = xml[lane], l2 = xml[64 + lane];
    const float mn = fmaxf(m_run, m2);
    const float a1 = __builtin_amdgcn_exp2f(m_run - mn), a2 = __builtin_amdgcn_exp2f(m2 - mn);
    const float inv = 1.f / (l_run * a1 + l2 * a2);
    const float s1 = a1 * inv, s2 = a2 * inv;
#pragma unroll
    for (int dd = 0; dd < 2; ++dd)
#pragma unroll
      for (int g = 0; g < 4; ++g) {
        const f32x4 v = *reinterpret_cast<const f32x4*>(xo + ((dd * 4 + g) * 64 + lane) * 4);
#pragma unroll
        for (int j = 0; j < 4; ++j) ow[dd * 4 + g][j] = (__bf16)((kh ? o[2 + dd][4 * g + j] : o[dd][4 * g + j]) * s1 + v[j] * s2);
      }
  }
  BARRIER();
  {
    char* ot = smem + xrow * 256;
#pragma unroll
    for (int dd = 0; dd < 2; ++dd)
#pragma unroll
      for (int g = 0; g < 4; ++g)
        *reinterpret_cast<bf16x4*>(ot + (((4 * (2 * kh + dd) + g) ^ (xrow & 15)) << 4) + h * 8) = ow[dd * 4 + g];
  }
  BARRIER();
  {
    const __amdgpu_buffer_rsrc_t ro = __builtin_amdgcn_make_buffer_rsrc(obuf + (size_t)b * SQ * (NH * DH), 0, SQ * NH * DH * 2, 0x00020000);
#pragma unroll
    for (int i = 0; i < 4; ++i) {
      const int idx = tid + i * 512;
      if (idx < SQ * 16) {
        const int row = idx >> 4, ch = idx & 15;
        const u32x4 v = *reinterpret_cast<const u32x4*>(smem + row * 256 + ((ch ^ (row & 15)) << 4));
        __builtin_amdgcn_raw_buffer_store_b128(v, ro, (hd * SQ + row) * 256 + ch * 16, 0, 16);
      }
    }
  }
  VMCNT(0);
  BARRIER();
  unsigned* ready = cnt + (64 + ob * 4 + ks) * 64;
  if (tid == 0) {
    __hip_atomic_fetch_add(cnt + (64 + b * 4 + (hd >> 3)) * 64, 1u, __ATOMIC_RELAXED, __HIP_MEMORY_SCOPE_AGENT);
    unsigned spins = 0;
    while (__hip_atomic_load(ready, __ATOMIC_RELAXED, __HIP_MEMORY_SCOPE_AGENT) < 8u && ++spins < (1u << 22)) __builtin_amdgcn_s_sleep(8);
    __builtin_amdgcn_fence(__ATOMIC_ACQUIRE, "agent");
    VMCNT(0);
  }
  BARRIER();

  const char* Ag = (const char*)(obuf + (size_t)ob * SQ * 4096 + (size_t)ks * 8 * SQ * DH);
  const int ar0 = 16 * wid + (lane >> 3), ar1 = ar0 + 8;
  const int aoff0 = min(ar0, SQ - 1) * 256 + ((wbc ^ ((ar0 >> 1) & 7)) << 4);
  const int aoff1 = min(ar1, SQ - 1) * 256 + ((wbc ^ ((ar1 >> 1) & 7)) << 4);
  auto dma_o = [&](int c) {
    const unsigned base = lds0 + (unsigned)(c & 3) * 16384u + (unsigned)wid * 2048u;
    glds16sc1(Ag + (c >> 1) * (SQ * 256) + (c & 1) * 128 + aoff0, base); glds16sc1(Ag + (c >> 1) * (SQ * 256) + (c & 1) * 128 + aoff1, base + 1024);
  };
  dma_o(0); dma_o(1); dma_o(2); dma_o(3); dma_wo(11, 11);
  f32x16 acc = {};
  bf16x8 af[2][4]; s16x4 glo[2][4], ghi[2][4];
  auto gload = [&](int c) {
    const char* sb = smem + (c & 3) * 16384;
    const unsigned bb = lds0 + 65536u + (unsigned)(c < 12 ? c : c - 12) * 8192u;
#pragma unroll
    for (int s = 0; s < 4; ++s) {
      af[c & 1][s] = *reinterpret_cast<const bf16x8*>(sb + xrow * 128 + (((2 * s + h) ^ ((xrow >> 1) & 7)) << 4));
      const int row0 = 16 * s + 8 * h + tq_;
      const unsigned a0 = bb + row0 * 128 + (((4 * kh + 2 * tg + (tp >> 1)) ^ (((row0 >> 1) & 1) << 2)) << 4) + (tp & 1) * 8;
      glo[c & 1][s] = tr_read(a0); ghi[c & 1][s] = tr_read(a0 + 512);
    }
    __builtin_amdgcn_sched_barrier(0);
  };
  auto gmma = [&](int c) {
    __builtin_amdgcn_sched_barrier(0);
#pragma unroll
    for (int s = 0; s < 4; ++s) acc = MFMA32(cat8(glo[c & 1][s], ghi[c & 1][s]), af[c & 1][s], acc);
  };
#define GSTEP(c, n) do { VMCNT(n); BARRIER(); gload(c); if ((c) >= 1 && (c) + 3 < 16) dma_o((c) + 3); if ((c) >= 1 && (c) <= 4) dma_wo((c) + 11, (c) - 1); if ((c) >= 1) gmma((c) - 1); } while (0)
  GSTEP(0, 7); GSTEP(1, 5); GSTEP(2, 6); GSTEP(3, 7); GSTEP(4, 7); GSTEP(5, 7); GSTEP(6, 6); GSTEP(7, 5);
  GSTEP(8, 4); GSTEP(9, 4); GSTEP(10, 4); GSTEP(11, 4); GSTEP(12, 4); GSTEP(13, 4); GSTEP(14, 2); GSTEP(15, 0);
  gmma(15);
#undef GSTEP
  BARRIER();
#pragma unroll
  for (int g = 0; g < 4; ++g) {
    f32x4 v = {acc[4 * g], acc[4 * g + 1], acc[4 * g + 2], acc[4 * g + 3]};
    *reinterpret_cast<f32x4*>(smem + xrow * 256 + (((8 * kh + 2 * g + h) ^ (xrow & 15)) << 4)) = v;
  }
  BARRIER();
  const int tile = ob * 8 + nt;
  const __amdgpu_buffer_rsrc_t rs = __builtin_amdgcn_make_buffer_rsrc(slabs + (size_t)tile * 4 * (128 * 64), 0, 4 * 128 * 64 * 4, 0x00020000);
#pragma unroll
  for (int i = 0; i < 4; ++i) {
    const int idx = tid + i * 512;
    if (idx < SQ * 16) {
      const int row = idx >> 4, ch = idx & 15;
      const u32x4 v = *reinterpret_cast<const u32x4*>(smem + row * 256 + ((ch ^ (row & 15)) << 4));
      __builtin_amdgcn_raw_buffer_store_b128(v, rs, ks * (128 * 64 * 4) + row * 256 + ch * 16, 0, 16);
    }
  }
  VMCNT(0);
  BARRIER();
  unsigned* flag = reinterpret_cast<unsigned*>(smem + 155648);
  if (tid == 0) *flag = __hip_atomic_fetch_add(cnt + tile * 64, 1u, __ATOMIC_RELAXED, __HIP_MEMORY_SCOPE_AGENT);
  BARRIER();
  if (*flag != 3) return;
  for (int idx = tid; idx < SQ * 16; idx += 512) {
    const int row = idx >> 4, c4 = (idx & 15) * 4;
    f32x4 v = __builtin_bit_cast(f32x4, __builtin_amdgcn_raw_buffer_load_b128(rs, (row * 64 + c4) * 4, 0, 16));
#pragma unroll
    for (int k = 1; k < 4; ++k)
      v += __builtin_bit_cast(f32x4, __builtin_amdgcn_raw_buffer_load_b128(rs, (k * (128 * 64) + row * 64 + c4) * 4, 0, 16));
    bf16x4 w;
#pragma unroll
    for (int j = 0; j < 4; ++j) w[j] = (__bf16)v[j];
    *reinterpret_cast<bf16x4*>(out + ((size_t)ob * SQ + row) * 512 + nt * 64 + c4) = w;
  }
}

extern "C" void kernel_launch(void* const* d_in, const int* in_sizes, int n_in, void* d_out, int out_size, void* d_ws,
                              size_t ws_size, hipStream_t stream) {
  const __bf16* x = (const __bf16*)d_in[0];
  const __bf16* kc = (const __bf16*)d_in[1];
  const __bf16* vc = (const __bf16*)d_in[2];
  const __bf16* wq = (const __bf16*)d_in[3];
  const __bf16* wo = (const __bf16*)d_in[4];
  unsigned* cnt = (unsigned*)d_ws;
  __bf16* obuf = (__bf16*)((char*)d_ws + 32768);
  float* slabs = (float*)((char*)d_ws + (16u << 20));
  hipMemsetAsync(cnt, 0, 96 * 256, stream);
  attn_kernel<<<NB * NH, 512, 0, stream>>>(x, kc, vc, wq, wo, obuf, slabs, cnt, (__bf16*)d_out);
}
```
